```python
import torch

def _softmax(x, axis=-1):
    return torch.softmax(x, dim=axis)
def _gelu(x):
    return torch.nn.functional.gelu(x, approximate="tanh")
def _silu(x):
    return torch.nn.functional.silu(x)
def _sigmoid(x):
    return torch.sigmoid(x)
def _layernorm(x, axis=-1, eps=1e-5):
    m = x.mean(dim=axis, keepdim=True)
    v = x.var(dim=axis, keepdim=True, unbiased=False)
    return (x - m) / torch.sqrt(v + eps)
def _scatter_add(tgt, idx, src, axis=0):
    return tgt.clone().index_add_(axis, idx, src)
def _segment_sum(src, seg, n):
    out = torch.zeros((n,) + src.shape[1:], dtype=src.dtype)
    return out.index_add_(0, seg, src)
def _pad(x, pw):
    flat = tuple(v for pair in reversed(pw) for v in pair)
    return torch.nn.functional.pad(x, flat)

kernel_name = 'standalone__normalfloat_lut_codebook_lookup__prompt'

def setup_inputs():

    _w = (lambda a: (lambda t: t.to(torch.bfloat16) if t.is_floating_point() else t)(torch.as_tensor(a)))
    B, S, D, F = 1, 2048, 1024, 3584
    G = 128 if 128 > 0 else D
    D = (D // G) * G
    ng = D // G
    nf4 = torch.tensor([
        -1.0, -0.6961928009986877, -0.5250730514526367, -0.39491748809814453,
        -0.28444138169288635, -0.18477343022823334, -0.09105003625154495, 0.0,
        0.07958029955625534, 0.16093020141124725, 0.24611230194568634, 0.33791524171829224,
        0.44070982933044434, 0.5626170039176941, 0.7229568362236023, 1.0
    ], dtype=float)
    qw = torch.randint(0, 16, (D, F)).to(torch.int64)
    absmax = (torch.abs((lambda *s: torch.randn(*s, dtype=torch.bfloat16) * 0.05)(ng, F)) + 0.01).to(float)
    return {
        "x": _w((lambda *s: torch.randn(*s, dtype=torch.bfloat16) * 0.05)(B, S, D)),
        "qw": _w(qw),
        "codebook": _w(nf4),
        "absmax": _w(absmax),
    }

def reference(x, qw, codebook, absmax):
    B, S, D = x.shape
    ng, F = absmax.shape
    G = D // ng
    w_nf = codebook[qw]
    s = (lambda x, n, axis: torch.repeat_interleave(x, n, dim=axis))(absmax, G, axis=0)
    w = (lambda x, dt: x.to(dt))(w_nf * s, x.dtype)
    return torch.einsum("bsd,df->bsf", x, w)
```

```cpp
#include <hip/hip_runtime.h>
#include <hip/hip_bf16.h>
typedef short bf16x8 __attribute__((ext_vector_type(8)));
typedef float f32x4 __attribute__((ext_vector_type(4)));
typedef unsigned short u16;

__global__ __launch_bounds__(256) void k_base(const u16* __restrict__ x, const int* __restrict__ qw, const u16* __restrict__ cb,
                        const u16* __restrict__ am, u16* __restrict__ out, int M, int N, int K, int G) {
  __shared__ __attribute__((aligned(16))) u16 sA[64][40];
  __shared__ __attribute__((aligned(16))) u16 sB[64][40];
  __shared__ float scb[16];
  int tid = threadIdx.x, lane = tid & 63, w = tid >> 6;
  if (tid < 16) scb[tid] = __uint_as_float(((unsigned)cb[tid]) << 16);
  int bm = blockIdx.y * 64, bn = blockIdx.x * 64;
  f32x4 acc[4] = {};
  __syncthreads();
  for (int k0 = 0; k0 < K; k0 += 32) {
    { int r = tid >> 2, c = (tid & 3) * 8;
      *(uint4*)&sA[r][c] = *(const uint4*)(x + (size_t)(bm + r) * K + k0 + c); }
    for (int e = tid; e < 32 * 64; e += 256) { int k = e >> 6, n = e & 63;
      int q = qw[(size_t)(k0 + k) * N + bn + n];
      float s = __uint_as_float(((unsigned)am[(size_t)((k0 + k) / G) * N + bn + n]) << 16);
      float v = scb[q] * s;
      __hip_bfloat16 b = __float2bfloat16(v);
      sB[n][k] = *(u16*)&b; }
    __syncthreads();
    bf16x8 a = *(const bf16x8*)&sA[w * 16 + (lane & 15)][(lane >> 4) * 8];
#pragma unroll
    for (int j = 0; j < 4; ++j) { bf16x8 b = *(const bf16x8*)&sB[j * 16 + (lane & 15)][(lane >> 4) * 8];
      acc[j] = __builtin_amdgcn_mfma_f32_16x16x32_bf16(a, b, acc[j], 0, 0, 0); }
    __syncthreads();
  }
#pragma unroll
  for (int j = 0; j < 4; ++j)
#pragma unroll
    for (int r = 0; r < 4; ++r) { __hip_bfloat16 b = __float2bfloat16(acc[j][r]);
      out[(size_t)(bm + w * 16 + (lane >> 4) * 4 + r) * N + bn + j * 16 + (lane & 15)] = *(u16*)&b; }
}

extern "C" void kernel_launch(void* const* d_in, const int* in_sizes, int n_in, void* d_out, int out_size, void* d_ws, size_t ws_size, hipStream_t stream) {
  const u16* x = (const u16*)d_in[0]; const int* qw = (const int*)d_in[1]; const u16* cb = (const u16*)d_in[2]; const u16* am = (const u16*)d_in[3];
  const int K = 1024, N = 3584, M = 2048, G = 128;
  k_base<<<dim3(N / 64, M / 64), 256, 0, stream>>>(x, qw, cb, am, (u16*)d_out, M, N, K, G);
}
```

```cpp
#include <hip/hip_runtime.h>
#include <stdint.h>
typedef unsigned short u16;
typedef __attribute__((ext_vector_type(8))) __bf16 bf16x8;
typedef __attribute__((ext_vector_type(2))) __bf16 bf16x2;
typedef float f32x4 __attribute__((ext_vector_type(4)));
typedef float f32x2 __attribute__((ext_vector_type(2)));
typedef int i32x4 __attribute__((ext_vector_type(4)));
typedef unsigned u32x4 __attribute__((ext_vector_type(4)));
typedef unsigned u32x2 __attribute__((ext_vector_type(2)));
#define LAS __attribute__((address_space(3)))

constexpr int M_ = 2048, N_ = 3584, K_ = 1024;
constexpr int BM = 128, BN = 224, BK = 64;
constexpr int XT_B = BM * BK * 2;
constexpr int WT_B = BN * BK * 2;
constexpr int SLOT_B = XT_B + WT_B;
constexpr int NSLOT = 3;
constexpr int LDS_B = NSLOT * SLOT_B;
constexpr int KB_B = BN * 16;
constexpr int J_B = (K_ / 8) * KB_B;
constexpr int OPITCH = 464;

__device__ __forceinline__ unsigned pk_bf16(float a, float b) {
  f32x2 v = {a, b};
  bf16x2 r = __builtin_convertvector(v, bf16x2);
  return __builtin_bit_cast(unsigned, r);
}
__device__ __forceinline__ float bf_lo(unsigned u) { return __uint_as_float(u << 16); }
__device__ __forceinline__ float bf_hi(unsigned u) { return __uint_as_float(u & 0xffff0000u); }

__global__ __launch_bounds__(512) void k_dequant(const int* __restrict__ qw, const u16* __restrict__ cb, const u16* __restrict__ am,
                                                 char* __restrict__ W) {
  __shared__ float lut[16];
  const int tid = threadIdx.x, lane = tid & 63, w = tid >> 6;
  const int j = blockIdx.x >> 4, kt = blockIdx.x & 15;
  const int cbk = lane < 56 ? lane : 55;
  const int kb = kt * 8 + w;
  const int* src = qw + (size_t)(kb * 8) * N_ + j * BN + cbk * 4;
  i32x4 q[8];
#pragma unroll
  for (int r = 0; r < 8; ++r) q[r] = __builtin_nontemporal_load((const i32x4*)(src + (size_t)r * N_));
  const u32x2 sc = *(const u32x2*)(am + (size_t)(kt >> 1) * N_ + j * BN + cbk * 4);
  if (tid < 16) lut[tid] = __uint_as_float((unsigned)cb[tid] << 16);
  __syncthreads();
  const float s[4] = {bf_lo(sc.x), bf_hi(sc.x), bf_lo(sc.y), bf_hi(sc.y)};
  char* dst = W + (size_t)j * J_B + (size_t)kb * KB_B + cbk * 64;
#pragma unroll
  for (int c = 0; c < 4; ++c) {
    u32x4 o;
#pragma unroll
    for (int p = 0; p < 4; ++p) o[p] = pk_bf16(lut[q[2 * p][c]] * s[c], lut[q[2 * p + 1][c]] * s[c]);
    if (lane < 56) *(u32x4*)(dst + c * 16) = o;
  }
}

#define WAIT_V(n) asm volatile("s_waitcnt vmcnt(%0)" ::"n"(n) : "memory")
#define WAIT_L(n) asm volatile("s_waitcnt lgkmcnt(%0)" ::"n"(n) : "memory")
#define WAIT_L0() __builtin_amdgcn_s_waitcnt(0xC07F)
#define BAR() do { asm volatile("" ::: "memory"); __builtin_amdgcn_s_barrier(); asm volatile("" ::: "memory"); } while (0)

__global__ __launch_bounds__(512, 2) void k_gemm(const u16* __restrict__ x, const char* __restrict__ W, u16* __restrict__ out) {
  extern __shared__ __attribute__((aligned(16))) char smem_g[];
  LAS char* smem = (LAS char*)smem_g;
  const int tid = threadIdx.x, lane = tid & 63;
  const int w = __builtin_amdgcn_readfirstlane(tid >> 6);
  const int b = blockIdx.x, xcd = b & 7, bi = b >> 3;
  const int mt = (xcd & 1) * 8 + (bi & 7), nt = (xcd >> 1) * 4 + (bi >> 3);
  const int m0 = mt * BM, n0 = nt * BN;

  if (w >= 4) {
    const int lw = w - 4;
    const __amdgpu_buffer_rsrc_t rx = __builtin_amdgcn_make_buffer_rsrc((void*)x, 0, M_ * K_ * 2, 0x00020000);
    const __amdgpu_buffer_rsrc_t rw = __builtin_amdgcn_make_buffer_rsrc((void*)W, 0, (N_ / BN) * J_B, 0x00020000);
    const int p = lane & 7, rl = lane >> 3, h = lane >> 4;
    int vx[2];
    const int vw = lane * 16;
#pragma unroll
    for (int par = 0; par < 2; ++par) {
      const int c = p ^ (4 * par + h);
      vx[par] = rl * (K_ * 2) + c * 16;
    }
    const int sx0 = (m0 + lw * 32) * (K_ * 2);
    const int sw0 = nt * J_B + lw * 7 * 1024;
    auto issue = [&](int kt, int slot) {
      LAS char* base = smem + slot * SLOT_B;
#pragma unroll
      for (int i = 0; i < 4; ++i)
        __builtin_amdgcn_raw_ptr_buffer_load_lds(rx, (LAS void*)(base + (lw * 4 + i) * 1024), 16, vx[i & 1], sx0 + i * 8 * (K_ * 2) + kt * 128, 0, 0);
#pragma unroll
      for (int i = 0; i < 7; ++i)
        __builtin_amdgcn_raw_ptr_buffer_load_lds(rw, (LAS void*)(base + XT_B + (lw * 7 + i) * 1024), 16, vw, sw0 + i * 1024 + kt * (8 * KB_B), 0, 0);
    };
    issue(0, 0);
    issue(1, 1);
    WAIT_V(11);
    BAR();
    int slot = 2;
#pragma unroll 1
    for (int t = 0; t < 14; ++t) {
      issue(t + 2, slot);
      slot = slot == 2 ? 0 : slot + 1;
      WAIT_V(11);
      BAR();
    }
    WAIT_V(0);
    BAR();
    BAR();
  } else {
    const int wm = w & 1, wn = w >> 1;
    const int fr = lane & 15, fh = lane >> 4;
    const int sw = (fh ^ (fr >> 1)) * 16;
    const int xa0 = (wm * 64 + fr) * 128 + sw;
    const int wa0 = XT_B + fh * KB_B + (wn * 112 + fr) * 16;
    f32x4 acc[4][7];
#pragma unroll
    for (int mi = 0; mi < 4; ++mi)
#pragma unroll
      for (int ni = 0; ni < 7; ++ni) acc[mi][ni] = (f32x4){0.f, 0.f, 0.f, 0.f};
    bf16x8 X0[4], W0[7], X1[4], W1[7];
#define RD(XF, WF, so, ks) do { \
    _Pragma("unroll") for (int ni = 0; ni < 7; ++ni) WF[ni] = *(const LAS bf16x8*)(smem + (so) + wa0 + (ks) * 4 * KB_B + ni * 256); \
    _Pragma("unroll") for (int mi = 0; mi < 4; ++mi) XF[mi] = *(const LAS bf16x8*)(smem + (so) + ((xa0 ^ ((ks) * 64))) + mi * 2048); } while (0)
#define MM(XF, WF) do { \
    _Pragma("unroll") for (int mi = 0; mi < 4; ++mi) _Pragma("unroll") for (int ni = 0; ni < 7; ++ni) \
      acc[mi][ni] = __builtin_amdgcn_mfma_f32_16x16x32_bf16(WF[ni], XF[mi], acc[mi][ni], 0, 0, 0); } while (0)
#define ILV() do { _Pragma("unroll") for (int g_ = 0; g_ < 11; ++g_) { \
      __builtin_amdgcn_sched_group_barrier(0x100, 1, 0); __builtin_amdgcn_sched_group_barrier(0x8, 2, 0); } \
      __builtin_amdgcn_sched_group_barrier(0x8, 6, 0); } while (0)
    BAR();
    int so = 0;
    RD(X0, W0, so, 0);
#pragma unroll 1
    for (int t = 0; t < 15; ++t) {
      WAIT_L0();
      __builtin_amdgcn_sched_barrier(0);
      RD(X1, W1, so, 1);
      MM(X0, W0);
      ILV();
      __builtin_amdgcn_sched_barrier(0);
      WAIT_L0();
      BAR();
      __builtin_amdgcn_sched_barrier(0);
      so = so == 2 * SLOT_B ? 0 : so + SLOT_B;
      RD(X0, W0, so, 0);
      MM(X1, W1);
      ILV();
      __builtin_amdgcn_sched_barrier(0);
    }
    WAIT_L0();
    __builtin_amdgcn_sched_barrier(0);
    RD(X1, W1, so, 1);
    MM(X0, W0);
    ILV();
    __builtin_amdgcn_sched_barrier(0);
    WAIT_L0();
    BAR();
    MM(X1, W1);
#pragma unroll
    for (int mi = 0; mi < 4; ++mi)
#pragma unroll
      for (int ni = 0; ni < 7; ++ni) {
        u32x2 o = {pk_bf16(acc[mi][ni][0], acc[mi][ni][1]), pk_bf16(acc[mi][ni][2], acc[mi][ni][3])};
        *(LAS u32x2*)(smem + (wm * 64 + mi * 16 + fr) * OPITCH + (wn * 112 + ni * 16 + fh * 4) * 2) = o;
      }
  }
  WAIT_L(0);
  BAR();
#pragma unroll
  for (int r = 0; r < 7; ++r) {
    const int c = r * 512 + tid;
    const int row = c / 28, ch = c - row * 28;
    const u32x4 v = *(const LAS u32x4*)(smem + row * OPITCH + ch * 16);
    __builtin_nontemporal_store(v, (u32x4*)((char*)out + (size_t)(m0 + row) * (N_ * 2) + n0 * 2 + ch * 16));
  }
}

extern "C" void kernel_launch(void* const* d_in, const int* in_sizes, int n_in, void* d_out, int out_size, void* d_ws, size_t ws_size,
                              hipStream_t stream) {
  const u16* x = (const u16*)d_in[0];
  const int* qw = (const int*)d_in[1];
  const u16* cb = (const u16*)d_in[2];
  const u16* am = (const u16*)d_in[3];
  char* W = (char*)d_ws;
  (void)hipFuncSetAttribute((const void*)k_gemm, hipFuncAttributeMaxDynamicSharedMemorySize, LDS_B);
  k_dequant<<<256, 512, 0, stream>>>(qw, cb, am, W);
  k_gemm<<<256, 512, LDS_B, stream>>>(x, W, (u16*)d_out);
}
```

```cpp
#include <hip/hip_runtime.h>
#include <stdint.h>
typedef unsigned short u16;
typedef __attribute__((ext_vector_type(8))) __bf16 bf16x8;
typedef __attribute__((ext_vector_type(2))) __bf16 bf16x2;
typedef float f32x4 __attribute__((ext_vector_type(4)));
typedef float f32x2 __attribute__((ext_vector_type(2)));
typedef int i32x4 __attribute__((ext_vector_type(4)));
typedef unsigned u32x4 __attribute__((ext_vector_type(4)));
typedef unsigned u32x2 __attribute__((ext_vector_type(2)));
#define LAS __attribute__((address_space(3)))

constexpr int M_ = 2048, N_ = 3584, K_ = 1024;
constexpr int BM = 128, BN = 224;
constexpr int XT_B = BM * 64 * 2;
constexpr int WT_B = BN * 64 * 2;
constexpr int SLOT_B = XT_B + WT_B;
constexpr int NSLOT = 3;
constexpr int KB_B = BN * 16;
constexpr int J_B = (K_ / 8) * KB_B;
constexpr int W_TOTAL = (N_ / BN) * J_B;
constexpr int FLAG_OFF = W_TOTAL;
constexpr int EPOCH_OFF = FLAG_OFF + 128 * 128;
constexpr int SCR_OFF = NSLOT * SLOT_B;
constexpr int SCR_W = 3712;
constexpr int DUMMY_OFF = SCR_OFF + 4 * SCR_W;
constexpr int LDS_B = DUMMY_OFF + 256;
constexpr int OPITCH = 464;

#define STAG 10

__device__ __forceinline__ unsigned pk_bf16(float a, float b) {
  f32x2 v = {a, b};
  bf16x2 r = __builtin_convertvector(v, bf16x2);
  return __builtin_bit_cast(unsigned, r);
}
__device__ __forceinline__ float bf_lo(unsigned u) { return __uint_as_float(u << 16); }
__device__ __forceinline__ float bf_hi(unsigned u) { return __uint_as_float(u & 0xffff0000u); }

#define WAIT_V(n) asm volatile("s_waitcnt vmcnt(%0)" ::"n"(n) : "memory")
#define WAIT_L0() __builtin_amdgcn_s_waitcnt(0xC07F)
#define BAR() do { asm volatile("" ::: "memory"); __builtin_amdgcn_s_barrier(); asm volatile("" ::: "memory"); } while (0)

#define STAMP(i)
#define DIAG_ARG

__global__ __launch_bounds__(512, 2) void k_fused(const u16* __restrict__ x, const int* __restrict__ qw, const u16* __restrict__ cb,
                                                  const u16* __restrict__ am, char* __restrict__ ws, u16* __restrict__ out DIAG_ARG) {
  extern __shared__ __attribute__((aligned(16))) char smem_g[];
  LAS char* smem = (LAS char*)smem_g;
  const int tid = threadIdx.x, lane = tid & 63;
  const int w = __builtin_amdgcn_readfirstlane(tid >> 6);
  const int b = blockIdx.x, xcd = b & 7, bi = b >> 3;
  const int mt = (xcd & 1) * 8 + (bi & 7), nt = (xcd >> 1) * 4 + (bi >> 3);
  const int m0 = mt * BM, n0 = nt * BN;
  STAMP(0);
  u32x2 E;
  {
    const char* pe = ws + EPOCH_OFF + nt * 128;
    asm volatile("global_load_dwordx2 %0, %1, off sc1" : "=v"(E) : "v"(pe) : "memory");
  }
  const __amdgpu_buffer_rsrc_t rw = __builtin_amdgcn_make_buffer_rsrc((void*)ws, 0, EPOCH_OFF + 16 * 128, 0x00020000);

  if (w >= 4) {
    const int lw = w - 4;
    const __amdgpu_buffer_rsrc_t rx = __builtin_amdgcn_make_buffer_rsrc((void*)x, 0, M_ * K_ * 2, 0x00020000);
    const int p = lane & 7, rl = lane >> 3, h = lane >> 4;
    int vx[2];
#pragma unroll
    for (int par = 0; par < 2; ++par) vx[par] = rl * (K_ * 2) + (p ^ (4 * par + h)) * 16;
    const int vw = lane * 16;
    const int sx0 = (m0 + lw * 32) * (K_ * 2);
    const int sw0 = nt * J_B + lw * 7 * 1024;
    auto issue_x = [&](int kt, int slot) {
      LAS char* base = smem + slot * SLOT_B;
#pragma unroll
      for (int i = 0; i < 4; ++i)
        __builtin_amdgcn_raw_ptr_buffer_load_lds(rx, (LAS void*)(base + (lw * 4 + i) * 1024), 16, vx[i & 1], sx0 + i * 8 * (K_ * 2) + kt * 128, 0, 0);
    };
    auto issue_w = [&](int kt, int slot) {
      LAS char* base = smem + slot * SLOT_B;
#pragma unroll
      for (int i = 0; i < 7; ++i)
        __builtin_amdgcn_raw_ptr_buffer_load_lds(rw, (LAS void*)(base + XT_B + (lw * 7 + i) * 1024), 16, vw, sw0 + i * 1024 + kt * (8 * KB_B), 0, 16);
    };
    const char* pf = ws + FLAG_OFF + nt * 8 * 128 + (lane & 15) * 8;
    u32x2 F;
#define POLL_ISSUE(g) do { const char* pp_ = pf + (g) * 128; asm volatile("global_load_dwordx2 %0, %1, off sc1" : "=v"(F) : "v"(pp_) : "memory"); } while (0)
#define POLL_OK() (__builtin_amdgcn_ballot_w64(F.x == tag.x && F.y == tag.y) == ~0ull)
#define PIN_F() asm volatile("" : "+v"(F))
    issue_x(0, 0);
    issue_x(1, 1);
    {
      const int vp = ((nt & 3) * 32 + (lane & 31)) * (K_ * 2) + (lane >> 5) * 128;
#pragma unroll
      for (int i = 0; i < 2; ++i) {
        const int ii = lw * 2 + i;
        if (ii < 7)
          __builtin_amdgcn_raw_ptr_buffer_load_lds(rx, (LAS void*)(smem + DUMMY_OFF), 4, vp, m0 * (K_ * 2) + (2 + 2 * ii) * 128, 0, 0);
      }
    }
    WAIT_V(0);
    asm volatile("" : "+v"(E));
    u32x2 tag;
    tag.x = E.x + 1u;
    tag.y = E.y ^ 0x5bd1e995u;
    STAMP(1);
    auto spin = [&](int g) {
      const unsigned long long t0 = __builtin_amdgcn_s_memrealtime();
      for (;;) {
        POLL_ISSUE(g);
        WAIT_V(0);
        PIN_F();
        if (POLL_OK()) break;
        if (__builtin_amdgcn_s_memrealtime() - t0 > 2000000ull) break;
        __builtin_amdgcn_s_sleep(1);
      }
    };
    spin(0);
    STAMP(2);
    POLL_ISSUE(1);
    issue_w(0, 0);
    issue_w(1, 1);
    WAIT_V(7);
    PIN_F();
    bool ok = POLL_OK();
    STAMP(3);
    BAR();
    if (!ok) spin(1);
    int slot = 2;
#pragma unroll 1
    for (int t = 0; t < 14; t += 2) {
      issue_x(t + 2, slot);
      issue_w(t + 2, slot);
      slot = slot == 2 ? 0 : slot + 1;
      WAIT_V(11);
      BAR();
      const int g = (t + 4) >> 1;
      POLL_ISSUE(g < 8 ? g : 7);
      issue_x(t + 3, slot);
      issue_w(t + 3, slot);
      slot = slot == 2 ? 0 : slot + 1;
      WAIT_V(11);
      PIN_F();
      ok = POLL_OK();
      BAR();
      if (!ok) spin(g < 8 ? g : 7);
      if (g == 7 && mt == 0 && lw == 0 && lane == 0) {
        char* pe = ws + EPOCH_OFF + nt * 128;
        u32x2 ne = {E.x + 1u, E.y};
        asm volatile("global_store_dwordx2 %0, %1, off sc1" ::"v"(pe), "v"(ne) : "memory");
      }
    }
    WAIT_V(0);
    BAR();
    BAR();
  } else {
    const int wm = w & 1, wn = w >> 1;
    const int cbk = lane < 56 ? lane : 55;
    LAS char* scr = smem + SCR_OFF + w * SCR_W;
    u32x2 scA, scB;
    unsigned cbv;
    i32x4 q[8];
    {
      const u16* pa = am + (size_t)w * N_ + n0 + cbk * 4;
      const u16* pb = pa + 4 * N_;
      const u16* pc = cb + (lane & 15);
      asm volatile("global_load_dwordx2 %0, %1, off" : "=v"(scA) : "v"(pa) : "memory");
      asm volatile("global_load_dwordx2 %0, %1, off" : "=v"(scB) : "v"(pb) : "memory");
      asm volatile("global_load_ushort %0, %1, off" : "=v"(cbv) : "v"(pc) : "memory");
    }
    for (int i = 0; i < w; ++i) __builtin_amdgcn_s_sleep(STAG);
    const int* src = qw + (size_t)(w * 128 + mt * 8) * N_ + n0 + cbk * 4;
#define RAW_LOAD(base) _Pragma("unroll") for (int r = 0; r < 8; ++r) { const int* p_ = (base) + (size_t)r * N_; \
      asm volatile("global_load_dwordx4 %0, %1, off nt" : "=v"(q[r]) : "v"(p_) : "memory"); }
    RAW_LOAD(src);
    WAIT_V(8);
    asm volatile("" : "+v"(E), "+v"(scA), "+v"(scB), "+v"(cbv));
    u32x2 tag;
    tag.x = E.x + 1u;
    tag.y = E.y ^ 0x5bd1e995u;
    if (lane < 16) *(LAS float*)(scr + 3584 + lane * 4) = __uint_as_float(cbv << 16);
    const LAS float* lut = (const LAS float*)(scr + 3584);
    auto produce = [&](const u32x2 sc, int g) {
#pragma unroll
      for (int r = 0; r < 8; ++r) asm volatile("" : "+v"(q[r]));
      const float s[4] = {bf_lo(sc.x), bf_hi(sc.x), bf_lo(sc.y), bf_hi(sc.y)};
#pragma unroll
      for (int c = 0; c < 4; ++c) {
        u32x4 o;
#pragma unroll
        for (int pp = 0; pp < 4; ++pp) o[pp] = pk_bf16(lut[q[2 * pp][c]] * s[c], lut[q[2 * pp + 1][c]] * s[c]);
        if (lane < 56) *(LAS u32x4*)(scr + lane * 64 + c * 16) = o;
      }
      if (g < 4) STAMP(9);
      const int so = nt * J_B + (g * 16 + mt) * KB_B;
#pragma unroll
      for (int s2 = 0; s2 < 4; ++s2) {
        const u32x4 v = *(const LAS u32x4*)(scr + s2 * 1024 + lane * 16);
        const int vo = (s2 == 3 && lane >= 32) ? 0x7ffffff0 : lane * 16;
        __builtin_amdgcn_raw_buffer_store_b128(v, rw, vo, so + s2 * 1024, 16);
      }
    };
    WAIT_V(0);
    STAMP(1);
    produce(scA, w);
    STAMP(10);
    RAW_LOAD(src + (size_t)512 * N_);
    STAMP(11);
    WAIT_V(8);
    STAMP(2);
    char* pfl = ws + FLAG_OFF + nt * 8 * 128 + mt * 8;
    if (lane == 0) { char* pp_ = pfl + w * 128; asm volatile("global_store_dwordx2 %0, %1, off sc1" ::"v"(pp_), "v"(tag) : "memory"); }
    WAIT_V(1);
    STAMP(3);
    produce(scB, w + 4);
    const int fr = lane & 15, fh = lane >> 4;
    const int xa0 = (wm * 64 + fr) * 128 + (fh ^ (fr >> 1)) * 16;
    const int wa0 = XT_B + fh * KB_B + (wn * 112 + fr) * 16;
    f32x4 acc[4][7];
#pragma unroll
    for (int mi = 0; mi < 4; ++mi)
#pragma unroll
      for (int ni = 0; ni < 7; ++ni) acc[mi][ni] = (f32x4){0.f, 0.f, 0.f, 0.f};
    bf16x8 X0[4], W0[7], X1[4], W1[7];
#define RD(XF, WF, so, ks) do { \
    _Pragma("unroll") for (int ni = 0; ni < 7; ++ni) WF[ni] = *(const LAS bf16x8*)(smem + (so) + wa0 + (ks) * 4 * KB_B + ni * 256); \
    _Pragma("unroll") for (int mi = 0; mi < 4; ++mi) XF[mi] = *(const LAS bf16x8*)(smem + (so) + ((xa0 ^ ((ks) * 64))) + mi * 2048); } while (0)
#define MM(XF, WF) do { \
    _Pragma("unroll") for (int mi = 0; mi < 4; ++mi) _Pragma("unroll") for (int ni = 0; ni < 7; ++ni) \
      acc[mi][ni] = __builtin_amdgcn_mfma_f32_16x16x32_bf16(WF[ni], XF[mi], acc[mi][ni], 0, 0, 0); } while (0)
#define ILV() do { _Pragma("unroll") for (int g_ = 0; g_ < 11; ++g_) { \
      __builtin_amdgcn_sched_group_barrier(0x100, 1, 0); __builtin_amdgcn_sched_group_barrier(0x8, 2, 0); } \
      __builtin_amdgcn_sched_group_barrier(0x8, 6, 0); } while (0)
    STAMP(4);
    BAR();
    STAMP(5);
    int so = 0;
    RD(X0, W0, so, 0);
#pragma unroll 1
    for (int t = 0; t < 15; ++t) {
      WAIT_L0();
      __builtin_amdgcn_sched_barrier(0);
      RD(X1, W1, so, 1);
      MM(X0, W0);
      ILV();
      __builtin_amdgcn_sched_barrier(0);
      WAIT_L0();
      if (t == 1) {
        WAIT_V(0);
        if (lane == 0) { char* pp_ = pfl + (w + 4) * 128; asm volatile("global_store_dwordx2 %0, %1, off sc1" ::"v"(pp_), "v"(tag) : "memory"); }
      }
      BAR();
      __builtin_amdgcn_sched_barrier(0);
      so = so == 2 * SLOT_B ? 0 : so + SLOT_B;
      RD(X0, W0, so, 0);
      MM(X1, W1);
      ILV();
      __builtin_amdgcn_sched_barrier(0);
    }
    WAIT_L0();
    __builtin_amdgcn_sched_barrier(0);
    RD(X1, W1, so, 1);
    MM(X0, W0);
    ILV();
    __builtin_amdgcn_sched_barrier(0);
    WAIT_L0();
    BAR();
    MM(X1, W1);
    STAMP(6);
#pragma unroll
    for (int mi = 0; mi < 4; ++mi)
#pragma unroll
      for (int ni = 0; ni < 7; ++ni) {
        u32x2 o = {pk_bf16(acc[mi][ni][0], acc[mi][ni][1]), pk_bf16(acc[mi][ni][2], acc[mi][ni][3])};
        *(LAS u32x2*)(smem + (wm * 64 + mi * 16 + fr) * OPITCH + (wn * 112 + ni * 16 + fh * 4) * 2) = o;
      }
  }
  WAIT_L0();
  BAR();
#pragma unroll
  for (int r = 0; r < 7; ++r) {
    const int c = r * 512 + tid;
    const int row = c / 28, ch = c - row * 28;
    const u32x4 v = *(const LAS u32x4*)(smem + row * OPITCH + ch * 16);
    __builtin_nontemporal_store(v, (u32x4*)((char*)out + (size_t)(m0 + row) * (N_ * 2) + n0 * 2 + ch * 16));
  }
  STAMP(7);
}

extern "C" void kernel_launch(void* const* d_in, const int* in_sizes, int n_in, void* d_out, int out_size, void* d_ws, size_t ws_size,
                              hipStream_t stream) {
  const u16* x = (const u16*)d_in[0];
  const int* qw = (const int*)d_in[1];
  const u16* cb = (const u16*)d_in[2];
  const u16* am = (const u16*)d_in[3];
  (void)hipFuncSetAttribute((const void*)k_fused, hipFuncAttributeMaxDynamicSharedMemorySize, LDS_B);
  k_fused<<<256, 512, LDS_B, stream>>>(x, qw, cb, am, (char*)d_ws, (u16*)d_out);
}
```

```cpp
#define SELFT
#define SELFB 0
#define TX 1
#define SCALE 20
#include <hip/hip_runtime.h>
#include <stdint.h>
typedef unsigned short u16;
typedef __attribute__((ext_vector_type(8))) __bf16 bf16x8;
typedef __attribute__((ext_vector_type(2))) __bf16 bf16x2;
typedef float f32x4 __attribute__((ext_vector_type(4)));
typedef float f32x2 __attribute__((ext_vector_type(2)));
typedef int i32x4 __attribute__((ext_vector_type(4)));
typedef unsigned u32x4 __attribute__((ext_vector_type(4)));
typedef unsigned u32x2 __attribute__((ext_vector_type(2)));
#define LAS __attribute__((address_space(3)))

constexpr int M_ = 2048, N_ = 3584, K_ = 1024;
constexpr int BM = 128, BN = 224;
constexpr int XT_B = BM * 64 * 2;
constexpr int WT_B = BN * 64 * 2;
constexpr int SLOT_B = XT_B + WT_B;
constexpr int NSLOT = 3;
constexpr int KB_B = BN * 16;
constexpr int J_B = (K_ / 8) * KB_B;
constexpr int W_TOTAL = (N_ / BN) * J_B;
constexpr int FLAG_OFF = W_TOTAL;
constexpr int EPOCH_OFF = FLAG_OFF + 128 * 128;
constexpr int SCR_OFF = NSLOT * SLOT_B;
constexpr int SCR_W = 3712;
constexpr int DUMMY_OFF = SCR_OFF + 4 * SCR_W;
constexpr int LDS_B = DUMMY_OFF + 256;
constexpr int OPITCH = 464;

#define STAG 10

__device__ __forceinline__ unsigned pk_bf16(float a, float b) {
  f32x2 v = {a, b};
  bf16x2 r = __builtin_convertvector(v, bf16x2);
  return __builtin_bit_cast(unsigned, r);
}
__device__ __forceinline__ float bf_lo(unsigned u) { return __uint_as_float(u << 16); }
__device__ __forceinline__ float bf_hi(unsigned u) { return __uint_as_float(u & 0xffff0000u); }

#define WAIT_V(n) asm volatile("s_waitcnt vmcnt(%0)" ::"n"(n) : "memory")
#define WAIT_L0() __builtin_amdgcn_s_waitcnt(0xC07F)
#define BAR() do { asm volatile("" ::: "memory"); __builtin_amdgcn_s_barrier(); asm volatile("" ::: "memory"); } while (0)

#define STAMP(i) do { stv[i] = __builtin_amdgcn_s_memrealtime(); } while (0)
#define DIAG_ARG

__global__ __launch_bounds__(512, 2) void k_fused(const u16* __restrict__ x, const int* __restrict__ qw, const u16* __restrict__ cb,
                                                  const u16* __restrict__ am, char* __restrict__ ws, u16* __restrict__ out DIAG_ARG) {
  extern __shared__ __attribute__((aligned(16))) char smem_g[];
  LAS char* smem = (LAS char*)smem_g;
  const int tid = threadIdx.x, lane = tid & 63;
  const int w = __builtin_amdgcn_readfirstlane(tid >> 6);
  const int b = blockIdx.x, xcd = b & 7, bi = b >> 3;
  const int mt = (xcd & 1) * 8 + (bi & 7), nt = (xcd >> 1) * 4 + (bi >> 3);
  const int m0 = mt * BM, n0 = nt * BN;
  unsigned long long stv[14] = {};
  STAMP(0);
  u32x2 E;
  {
    const char* pe = ws + EPOCH_OFF + nt * 128;
    asm volatile("global_load_dwordx2 %0, %1, off sc1" : "=v"(E) : "v"(pe) : "memory");
  }
  const __amdgpu_buffer_rsrc_t rw = __builtin_amdgcn_make_buffer_rsrc((void*)ws, 0, EPOCH_OFF + 16 * 128, 0x00020000);

  if (w >= 4) {
    const int lw = w - 4;
    const __amdgpu_buffer_rsrc_t rx = __builtin_amdgcn_make_buffer_rsrc((void*)x, 0, M_ * K_ * 2, 0x00020000);
    const int p = lane & 7, rl = lane >> 3, h = lane >> 4;
    int vx[2];
#pragma unroll
    for (int par = 0; par < 2; ++par) vx[par] = rl * (K_ * 2) + (p ^ (4 * par + h)) * 16;
    const int vw = lane * 16;
    const int sx0 = (m0 + lw * 32) * (K_ * 2);
    const int sw0 = nt * J_B + lw * 7 * 1024;
    auto issue_x = [&](int kt, int slot) {
      LAS char* base = smem + slot * SLOT_B;
#pragma unroll
      for (int i = 0; i < 4; ++i)
        __builtin_amdgcn_raw_ptr_buffer_load_lds(rx, (LAS void*)(base + (lw * 4 + i) * 1024), 16, vx[i & 1], sx0 + i * 8 * (K_ * 2) + kt * 128, 0, 0);
    };
    auto issue_w = [&](int kt, int slot) {
      LAS char* base = smem + slot * SLOT_B;
#pragma unroll
      for (int i = 0; i < 7; ++i)
        __builtin_amdgcn_raw_ptr_buffer_load_lds(rw, (LAS void*)(base + XT_B + (lw * 7 + i) * 1024), 16, vw, sw0 + i * 1024 + kt * (8 * KB_B), 0, 16);
    };
    const char* pf = ws + FLAG_OFF + nt * 8 * 128 + (lane & 15) * 8;
    u32x2 F;
#define POLL_ISSUE(g) do { const char* pp_ = pf + (g) * 128; asm volatile("global_load_dwordx2 %0, %1, off sc1" : "=v"(F) : "v"(pp_) : "memory"); } while (0)
#define POLL_OK() (__builtin_amdgcn_ballot_w64(F.x == tag.x && F.y == tag.y) == ~0ull)
#define PIN_F() asm volatile("" : "+v"(F))
    issue_x(0, 0);
    issue_x(1, 1);
    {
      const int vp = ((nt & 3) * 32 + (lane & 31)) * (K_ * 2) + (lane >> 5) * 128;
#pragma unroll
      for (int i = 0; i < 2; ++i) {
        const int ii = lw * 2 + i;
        if (ii < 7)
          __builtin_amdgcn_raw_ptr_buffer_load_lds(rx, (LAS void*)(smem + DUMMY_OFF), 4, vp, m0 * (K_ * 2) + (2 + 2 * ii) * 128, 0, 0);
      }
    }
    WAIT_V(0);
    asm volatile("" : "+v"(E));
    u32x2 tag;
    tag.x = E.x + 1u;
    tag.y = E.y ^ 0x5bd1e995u;
    STAMP(1);
    auto spin = [&](int g) {
      const unsigned long long t0 = __builtin_amdgcn_s_memrealtime();
      for (;;) {
        POLL_ISSUE(g);
        WAIT_V(0);
        PIN_F();
        if (POLL_OK()) break;
        if (__builtin_amdgcn_s_memrealtime() - t0 > 2000000ull) break;
        __builtin_amdgcn_s_sleep(1);
      }
    };
    spin(0);
    STAMP(2);
    POLL_ISSUE(1);
    issue_w(0, 0);
    issue_w(1, 1);
    WAIT_V(7);
    PIN_F();
    bool ok = POLL_OK();
    STAMP(3);
    BAR();
    if (!ok) spin(1);
    int slot = 2;
#pragma unroll 1
    for (int t = 0; t < 14; t += 2) {
      issue_x(t + 2, slot);
      issue_w(t + 2, slot);
      slot = slot == 2 ? 0 : slot + 1;
      WAIT_V(11);
      BAR();
      const int g = (t + 4) >> 1;
      POLL_ISSUE(g < 8 ? g : 7);
      issue_x(t + 3, slot);
      issue_w(t + 3, slot);
      slot = slot == 2 ? 0 : slot + 1;
      WAIT_V(11);
      PIN_F();
      ok = POLL_OK();
      BAR();
      if (!ok) spin(g < 8 ? g : 7);
      if (g == 7 && mt == 0 && lw == 0 && lane == 0) {
        char* pe = ws + EPOCH_OFF + nt * 128;
        u32x2 ne = {E.x + 1u, E.y};
        asm volatile("global_store_dwordx2 %0, %1, off sc1" ::"v"(pe), "v"(ne) : "memory");
      }
    }
    WAIT_V(0);
    BAR();
    BAR();
  } else {
    const int wm = w & 1, wn = w >> 1;
    const int cbk = lane < 56 ? lane : 55;
    LAS char* scr = smem + SCR_OFF + w * SCR_W;
    u32x2 scA, scB;
    unsigned cbv;
    i32x4 q[8];
    {
      const u16* pa = am + (size_t)w * N_ + n0 + cbk * 4;
      const u16* pb = pa + 4 * N_;
      const u16* pc = cb + (lane & 15);
      asm volatile("global_load_dwordx2 %0, %1, off" : "=v"(scA) : "v"(pa) : "memory");
      asm volatile("global_load_dwordx2 %0, %1, off" : "=v"(scB) : "v"(pb) : "memory");
      asm volatile("global_load_ushort %0, %1, off" : "=v"(cbv) : "v"(pc) : "memory");
    }
    for (int i = 0; i < w; ++i) __builtin_amdgcn_s_sleep(STAG);
    const int* src = qw + (size_t)(w * 128 + mt * 8) * N_ + n0 + cbk * 4;
#define RAW_LOAD(base) _Pragma("unroll") for (int r = 0; r < 8; ++r) { const int* p_ = (base) + (size_t)r * N_; \
      asm volatile("global_load_dwordx4 %0, %1, off nt" : "=v"(q[r]) : "v"(p_) : "memory"); }
    RAW_LOAD(src);
    WAIT_V(8);
    asm volatile("" : "+v"(E), "+v"(scA), "+v"(scB), "+v"(cbv));
    u32x2 tag;
    tag.x = E.x + 1u;
    tag.y = E.y ^ 0x5bd1e995u;
    if (lane < 16) *(LAS float*)(scr + 3584 + lane * 4) = __uint_as_float(cbv << 16);
    const LAS float* lut = (const LAS float*)(scr + 3584);
    auto produce = [&](const u32x2 sc, int g) {
#pragma unroll
      for (int r = 0; r < 8; ++r) asm volatile("" : "+v"(q[r]));
      const float s[4] = {bf_lo(sc.x), bf_hi(sc.x), bf_lo(sc.y), bf_hi(sc.y)};
#pragma unroll
      for (int c = 0; c < 4; ++c) {
        u32x4 o;
#pragma unroll
        for (int pp = 0; pp < 4; ++pp) o[pp] = pk_bf16(lut[q[2 * pp][c]] * s[c], lut[q[2 * pp + 1][c]] * s[c]);
        if (lane < 56) *(LAS u32x4*)(scr + lane * 64 + c * 16) = o;
      }
      if (g < 4) STAMP(9);
      const int so = nt * J_B + (g * 16 + mt) * KB_B;
#pragma unroll
      for (int s2 = 0; s2 < 4; ++s2) {
        const u32x4 v = *(const LAS u32x4*)(scr + s2 * 1024 + lane * 16);
        const int vo = (s2 == 3 && lane >= 32) ? 0x7ffffff0 : lane * 16;
        __builtin_amdgcn_raw_buffer_store_b128(v, rw, vo, so + s2 * 1024, 16);
      }
    };
    WAIT_V(0);
    STAMP(1);
    produce(scA, w);
    STAMP(10);
    RAW_LOAD(src + (size_t)512 * N_);
    STAMP(11);
    WAIT_V(8);
    STAMP(2);
    char* pfl = ws + FLAG_OFF + nt * 8 * 128 + mt * 8;
    if (lane == 0) { char* pp_ = pfl + w * 128; asm volatile("global_store_dwordx2 %0, %1, off sc1" ::"v"(pp_), "v"(tag) : "memory"); }
    WAIT_V(1);
    STAMP(3);
    produce(scB, w + 4);
    const int fr = lane & 15, fh = lane >> 4;
    const int xa0 = (wm * 64 + fr) * 128 + (fh ^ (fr >> 1)) * 16;
    const int wa0 = XT_B + fh * KB_B + (wn * 112 + fr) * 16;
    f32x4 acc[4][7];
#pragma unroll
    for (int mi = 0; mi < 4; ++mi)
#pragma unroll
      for (int ni = 0; ni < 7; ++ni) acc[mi][ni] = (f32x4){0.f, 0.f, 0.f, 0.f};
    bf16x8 X0[4], W0[7], X1[4], W1[7];
#define RD(XF, WF, so, ks) do { \
    _Pragma("unroll") for (int ni = 0; ni < 7; ++ni) WF[ni] = *(const LAS bf16x8*)(smem + (so) + wa0 + (ks) * 4 * KB_B + ni * 256); \
    _Pragma("unroll") for (int mi = 0; mi < 4; ++mi) XF[mi] = *(const LAS bf16x8*)(smem + (so) + ((xa0 ^ ((ks) * 64))) + mi * 2048); } while (0)
#define MM(XF, WF) do { \
    _Pragma("unroll") for (int mi = 0; mi < 4; ++mi) _Pragma("unroll") for (int ni = 0; ni < 7; ++ni) \
      acc[mi][ni] = __builtin_amdgcn_mfma_f32_16x16x32_bf16(WF[ni], XF[mi], acc[mi][ni], 0, 0, 0); } while (0)
#define ILV() do { _Pragma("unroll") for (int g_ = 0; g_ < 11; ++g_) { \
      __builtin_amdgcn_sched_group_barrier(0x100, 1, 0); __builtin_amdgcn_sched_group_barrier(0x8, 2, 0); } \
      __builtin_amdgcn_sched_group_barrier(0x8, 6, 0); } while (0)
    STAMP(4);
    BAR();
    STAMP(5);
    int so = 0;
    RD(X0, W0, so, 0);
#pragma unroll 1
    for (int t = 0; t < 15; ++t) {
      WAIT_L0();
      __builtin_amdgcn_sched_barrier(0);
      RD(X1, W1, so, 1);
      MM(X0, W0);
      ILV();
      __builtin_amdgcn_sched_barrier(0);
      WAIT_L0();
      if (t == 1) {
        WAIT_V(0);
        if (lane == 0) { char* pp_ = pfl + (w + 4) * 128; asm volatile("global_store_dwordx2 %0, %1, off sc1" ::"v"(pp_), "v"(tag) : "memory"); }
      }
      BAR();
      __builtin_amdgcn_sched_barrier(0);
      so = so == 2 * SLOT_B ? 0 : so + SLOT_B;
      RD(X0, W0, so, 0);
      MM(X1, W1);
      ILV();
      __builtin_amdgcn_sched_barrier(0);
    }
    WAIT_L0();
    __builtin_amdgcn_sched_barrier(0);
    RD(X1, W1, so, 1);
    MM(X0, W0);
    ILV();
    __builtin_amdgcn_sched_barrier(0);
    WAIT_L0();
    BAR();
    MM(X1, W1);
    STAMP(6);
#pragma unroll
    for (int mi = 0; mi < 4; ++mi)
#pragma unroll
      for (int ni = 0; ni < 7; ++ni) {
        u32x2 o = {pk_bf16(acc[mi][ni][0], acc[mi][ni][1]), pk_bf16(acc[mi][ni][2], acc[mi][ni][3])};
        *(LAS u32x2*)(smem + (wm * 64 + mi * 16 + fr) * OPITCH + (wn * 112 + ni * 16 + fh * 4) * 2) = o;
      }
  }
  WAIT_L0();
  BAR();
#pragma unroll
  for (int r = 0; r < 7; ++r) {
    const int c = r * 512 + tid;
    const int row = c / 28, ch = c - row * 28;
    const u32x4 v = *(const LAS u32x4*)(smem + row * OPITCH + ch * 16);
    __builtin_nontemporal_store(v, (u32x4*)((char*)out + (size_t)(m0 + row) * (N_ * 2) + n0 * 2 + ch * 16));
  }
  STAMP(7);
  if (b == SELFB && w == 0) {
    const unsigned long long d = (stv[TX] - stv[0]) * SCALE, t1 = __builtin_amdgcn_s_memrealtime();
    while (__builtin_amdgcn_s_memrealtime() - t1 < d) __builtin_amdgcn_s_sleep(1);
  }
}

extern "C" void kernel_launch(void* const* d_in, const int* in_sizes, int n_in, void* d_out, int out_size, void* d_ws, size_t ws_size,
                              hipStream_t stream) {
  const u16* x = (const u16*)d_in[0];
  const int* qw = (const int*)d_in[1];
  const u16* cb = (const u16*)d_in[2];
  const u16* am = (const u16*)d_in[3];
  (void)hipFuncSetAttribute((const void*)k_fused, hipFuncAttributeMaxDynamicSharedMemorySize, LDS_B);
  k_fused<<<256, 512, LDS_B, stream>>>(x, qw, cb, am, (char*)d_ws, (u16*)d_out);
}
```

```cpp
#define STAG 20
#include <hip/hip_runtime.h>
#include <stdint.h>
typedef unsigned short u16;
typedef __attribute__((ext_vector_type(8))) __bf16 bf16x8;
typedef __attribute__((ext_vector_type(2))) __bf16 bf16x2;
typedef float f32x4 __attribute__((ext_vector_type(4)));
typedef float f32x2 __attribute__((ext_vector_type(2)));
typedef int i32x4 __attribute__((ext_vector_type(4)));
typedef unsigned u32x4 __attribute__((ext_vector_type(4)));
typedef unsigned u32x2 __attribute__((ext_vector_type(2)));
#define LAS __attribute__((address_space(3)))

constexpr int M_ = 2048, N_ = 3584, K_ = 1024;
constexpr int BM = 128, BN = 224;
constexpr int XT_B = BM * 64 * 2;
constexpr int WT_B = BN * 64 * 2;
constexpr int SLOT_B = XT_B + WT_B;
constexpr int NSLOT = 3;
constexpr int KB_B = BN * 16;
constexpr int J_B = (K_ / 8) * KB_B;
constexpr int W_TOTAL = (N_ / BN) * J_B;
constexpr int FLAG_OFF = W_TOTAL;
constexpr int EPOCH_OFF = FLAG_OFF + 128 * 128;
constexpr int SCR_OFF = NSLOT * SLOT_B;
constexpr int SCR_W = 3712;
constexpr int DUMMY_OFF = SCR_OFF + 4 * SCR_W;
constexpr int LDS_B = DUMMY_OFF + 256;
constexpr int OPITCH = 464;

__device__ __forceinline__ unsigned pk_bf16(float a, float b) {
  f32x2 v = {a, b};
  bf16x2 r = __builtin_convertvector(v, bf16x2);
  return __builtin_bit_cast(unsigned, r);
}
__device__ __forceinline__ float bf_lo(unsigned u) { return __uint_as_float(u << 16); }
__device__ __forceinline__ float bf_hi(unsigned u) { return __uint_as_float(u & 0xffff0000u); }

#define WAIT_V(n) asm volatile("s_waitcnt vmcnt(%0)" ::"n"(n) : "memory")
#define WAIT_L0() __builtin_amdgcn_s_waitcnt(0xC07F)
#define BAR() do { asm volatile("" ::: "memory"); __builtin_amdgcn_s_barrier(); asm volatile("" ::: "memory"); } while (0)

#define STAMP(i)
#define DIAG_ARG

__global__ __launch_bounds__(512, 2) void k_fused(const u16* __restrict__ x, const int* __restrict__ qw, const u16* __restrict__ cb,
                                                  const u16* __restrict__ am, char* __restrict__ ws, u16* __restrict__ out DIAG_ARG) {
  extern __shared__ __attribute__((aligned(16))) char smem_g[];
  LAS char* smem = (LAS char*)smem_g;
  const int tid = threadIdx.x, lane = tid & 63;
  const int w = __builtin_amdgcn_readfirstlane(tid >> 6);
  const int b = blockIdx.x, xcd = b & 7, bi = b >> 3;
  const int mt = (xcd & 1) * 8 + (bi & 7), nt = (xcd >> 1) * 4 + (bi >> 3);
  const int m0 = mt * BM, n0 = nt * BN;
  STAMP(0);
  u32x2 E;
  {
    const char* pe = ws + EPOCH_OFF + nt * 128;
    asm volatile("global_load_dwordx2 %0, %1, off sc1" : "=v"(E) : "v"(pe) : "memory");
  }
  const __amdgpu_buffer_rsrc_t rw = __builtin_amdgcn_make_buffer_rsrc((void*)ws, 0, EPOCH_OFF + 16 * 128, 0x00020000);

  if (w >= 4) {
    const int lw = w - 4;
    const __amdgpu_buffer_rsrc_t rx = __builtin_amdgcn_make_buffer_rsrc((void*)x, 0, M_ * K_ * 2, 0x00020000);
    const int p = lane & 7, rl = lane >> 3, h = lane >> 4;
    int vx[2];
#pragma unroll
    for (int par = 0; par < 2; ++par) vx[par] = rl * (K_ * 2) + (p ^ (4 * par + h)) * 16;
    const int vw = lane * 16;
    const int sx0 = (m0 + lw * 32) * (K_ * 2);
    const int sw0 = nt * J_B + lw * 7 * 1024;
    auto issue_x = [&](int kt, int slot) {
      LAS char* base = smem + slot * SLOT_B;
#pragma unroll
      for (int i = 0; i < 4; ++i)
        __builtin_amdgcn_raw_ptr_buffer_load_lds(rx, (LAS void*)(base + (lw * 4 + i) * 1024), 16, vx[i & 1], sx0 + i * 8 * (K_ * 2) + kt * 128, 0, 0);
    };
    auto issue_w = [&](int kt, int slot) {
      LAS char* base = smem + slot * SLOT_B;
#pragma unroll
      for (int i = 0; i < 7; ++i)
        __builtin_amdgcn_raw_ptr_buffer_load_lds(rw, (LAS void*)(base + XT_B + (lw * 7 + i) * 1024), 16, vw, sw0 + i * 1024 + kt * (8 * KB_B), 0, 16);
    };
    const char* pf = ws + FLAG_OFF + nt * 8 * 128 + (lane & 15) * 8;
    u32x2 F;
#define POLL_ISSUE(g) do { const char* pp_ = pf + (g) * 128; asm volatile("global_load_dwordx2 %0, %1, off sc1" : "=v"(F) : "v"(pp_) : "memory"); } while (0)
#define POLL_OK() (__builtin_amdgcn_ballot_w64(F.x == tag.x && F.y == tag.y) == ~0ull)
#define PIN_F() asm volatile("" : "+v"(F))
    issue_x(0, 0);
    issue_x(1, 1);
    {
      const int vp = ((nt & 3) * 32 + (lane & 31)) * (K_ * 2) + (lane >> 5) * 128;
#pragma unroll
      for (int i = 0; i < 2; ++i) {
        const int ii = lw * 2 + i;
        if (ii < 7)
          __builtin_amdgcn_raw_ptr_buffer_load_lds(rx, (LAS void*)(smem + DUMMY_OFF), 4, vp, m0 * (K_ * 2) + (2 + 2 * ii) * 128, 0, 0);
      }
    }
    WAIT_V(0);
    asm volatile("" : "+v"(E));
    u32x2 tag;
    tag.x = E.x + 1u;
    tag.y = E.y ^ 0x5bd1e995u;
    STAMP(1);
    auto spin = [&](int g) {
      const unsigned long long t0 = __builtin_amdgcn_s_memrealtime();
      for (;;) {
        POLL_ISSUE(g);
        WAIT_V(0);
        PIN_F();
        if (POLL_OK()) break;
        if (__builtin_amdgcn_s_memrealtime() - t0 > 2000000ull) break;
        __builtin_amdgcn_s_sleep(1);
      }
    };
    spin(0);
    STAMP(2);
    POLL_ISSUE(1);
    issue_w(0, 0);
    issue_w(1, 1);
    WAIT_V(7);
    PIN_F();
    bool ok = POLL_OK();
    STAMP(3);
    BAR();
    if (!ok) spin(1);
    int slot = 2;
#pragma unroll 1
    for (int t = 0; t < 14; t += 2) {
      issue_x(t + 2, slot);
      issue_w(t + 2, slot);
      slot = slot == 2 ? 0 : slot + 1;
      WAIT_V(11);
      BAR();
      const int g = (t + 4) >> 1;
      POLL_ISSUE(g < 8 ? g : 7);
      issue_x(t + 3, slot);
      issue_w(t + 3, slot);
      slot = slot == 2 ? 0 : slot + 1;
      WAIT_V(11);
      PIN_F();
      ok = POLL_OK();
      BAR();
      if (!ok) spin(g < 8 ? g : 7);
      if (g == 7 && mt == 0 && lw == 0 && lane == 0) {
        char* pe = ws + EPOCH_OFF + nt * 128;
        u32x2 ne = {E.x + 1u, E.y};
        asm volatile("global_store_dwordx2 %0, %1, off sc1" ::"v"(pe), "v"(ne) : "memory");
      }
    }
    WAIT_V(0);
    BAR();
    BAR();
  } else {
    const int wm = w & 1, wn = w >> 1;
    const int cbk = lane < 56 ? lane : 55;
    LAS char* scr = smem + SCR_OFF + w * SCR_W;
    u32x2 scA, scB;
    unsigned cbv;
    i32x4 q[8];
    {
      const u16* pa = am + (size_t)w * N_ + n0 + cbk * 4;
      const u16* pb = pa + 4 * N_;
      const u16* pc = cb + (lane & 15);
      asm volatile("global_load_dwordx2 %0, %1, off" : "=v"(scA) : "v"(pa) : "memory");
      asm volatile("global_load_dwordx2 %0, %1, off" : "=v"(scB) : "v"(pb) : "memory");
      asm volatile("global_load_ushort %0, %1, off" : "=v"(cbv) : "v"(pc) : "memory");
    }
    for (int i = 0; i < w; ++i) __builtin_amdgcn_s_sleep(STAG);
    const int* src = qw + (size_t)(w * 128 + mt * 8) * N_ + n0 + cbk * 4;
#define RAW_LOAD(base) _Pragma("unroll") for (int r = 0; r < 8; ++r) { const int* p_ = (base) + (size_t)r * N_; \
      asm volatile("global_load_dwordx4 %0, %1, off nt" : "=v"(q[r]) : "v"(p_) : "memory"); }
    RAW_LOAD(src);
    WAIT_V(8);
    asm volatile("" : "+v"(E), "+v"(scA), "+v"(scB), "+v"(cbv));
    u32x2 tag;
    tag.x = E.x + 1u;
    tag.y = E.y ^ 0x5bd1e995u;
    if (lane < 16) *(LAS float*)(scr + 3584 + lane * 4) = __uint_as_float(cbv << 16);
    const LAS float* lut = (const LAS float*)(scr + 3584);
    auto produce = [&](const u32x2 sc, int g) {
#pragma unroll
      for (int r = 0; r < 8; ++r) asm volatile("" : "+v"(q[r]));
      const float s[4] = {bf_lo(sc.x), bf_hi(sc.x), bf_lo(sc.y), bf_hi(sc.y)};
#pragma unroll
      for (int c = 0; c < 4; ++c) {
        u32x4 o;
#pragma unroll
        for (int pp = 0; pp < 4; ++pp) o[pp] = pk_bf16(lut[q[2 * pp][c]] * s[c], lut[q[2 * pp + 1][c]] * s[c]);
        if (lane < 56) *(LAS u32x4*)(scr + lane * 64 + c * 16) = o;
      }
      if (g < 4) STAMP(9);
      const int so = nt * J_B + (g * 16 + mt) * KB_B;
#pragma unroll
      for (int s2 = 0; s2 < 4; ++s2) {
        const u32x4 v = *(const LAS u32x4*)(scr + s2 * 1024 + lane * 16);
        const int vo = (s2 == 3 && lane >= 32) ? 0x7ffffff0 : lane * 16;
        __builtin_amdgcn_raw_buffer_store_b128(v, rw, vo, so + s2 * 1024, 16);
      }
    };
    WAIT_V(0);
    STAMP(1);
    produce(scA, w);
    STAMP(10);
    RAW_LOAD(src + (size_t)512 * N_);
    STAMP(11);
    WAIT_V(8);
    STAMP(2);
    char* pfl = ws + FLAG_OFF + nt * 8 * 128 + mt * 8;
    if (lane == 0) { char* pp_ = pfl + w * 128; asm volatile("global_store_dwordx2 %0, %1, off sc1" ::"v"(pp_), "v"(tag) : "memory"); }
    WAIT_V(1);
    STAMP(3);
    produce(scB, w + 4);
    const int fr = lane & 15, fh = lane >> 4;
    const int xa0 = (wm * 64 + fr) * 128 + (fh ^ (fr >> 1)) * 16;
    const int wa0 = XT_B + fh * KB_B + (wn * 112 + fr) * 16;
    f32x4 acc[4][7];
#pragma unroll
    for (int mi = 0; mi < 4; ++mi)
#pragma unroll
      for (int ni = 0; ni < 7; ++ni) acc[mi][ni] = (f32x4){0.f, 0.f, 0.f, 0.f};
    bf16x8 X0[4], W0[7], X1[4], W1[7];
#define RD(XF, WF, so, ks) do { \
    _Pragma("unroll") for (int ni = 0; ni < 7; ++ni) WF[ni] = *(const LAS bf16x8*)(smem + (so) + wa0 + (ks) * 4 * KB_B + ni * 256); \
    _Pragma("unroll") for (int mi = 0; mi < 4; ++mi) XF[mi] = *(const LAS bf16x8*)(smem + (so) + ((xa0 ^ ((ks) * 64))) + mi * 2048); } while (0)
#define MM(XF, WF) do { \
    _Pragma("unroll") for (int mi = 0; mi < 4; ++mi) _Pragma("unroll") for (int ni = 0; ni < 7; ++ni) \
      acc[mi][ni] = __builtin_amdgcn_mfma_f32_16x16x32_bf16(WF[ni], XF[mi], acc[mi][ni], 0, 0, 0); } while (0)
#define ILV() do { _Pragma("unroll") for (int g_ = 0; g_ < 11; ++g_) { \
      __builtin_amdgcn_sched_group_barrier(0x100, 1, 0); __builtin_amdgcn_sched_group_barrier(0x8, 2, 0); } \
      __builtin_amdgcn_sched_group_barrier(0x8, 6, 0); } while (0)
    STAMP(4);
    BAR();
    STAMP(5);
    int so = 0;
    RD(X0, W0, so, 0);
#pragma unroll 1
    for (int t = 0; t < 15; ++t) {
      WAIT_L0();
      __builtin_amdgcn_sched_barrier(0);
      RD(X1, W1, so, 1);
      MM(X0, W0);
      ILV();
      __builtin_amdgcn_sched_barrier(0);
      WAIT_L0();
      if (t == 1) {
        WAIT_V(0);
        if (lane == 0) { char* pp_ = pfl + (w + 4) * 128; asm volatile("global_store_dwordx2 %0, %1, off sc1" ::"v"(pp_), "v"(tag) : "memory"); }
      }
      BAR();
      __builtin_amdgcn_sched_barrier(0);
      so = so == 2 * SLOT_B ? 0 : so + SLOT_B;
      RD(X0, W0, so, 0);
      MM(X1, W1);
      ILV();
      __builtin_amdgcn_sched_barrier(0);
    }
    WAIT_L0();
    __builtin_amdgcn_sched_barrier(0);
    RD(X1, W1, so, 1);
    MM(X0, W0);
    ILV();
    __builtin_amdgcn_sched_barrier(0);
    WAIT_L0();
    BAR();
    MM(X1, W1);
    STAMP(6);
#pragma unroll
    for (int mi = 0; mi < 4; ++mi)
#pragma unroll
      for (int ni = 0; ni < 7; ++ni) {
        u32x2 o = {pk_bf16(acc[mi][ni][0], acc[mi][ni][1]), pk_bf16(acc[mi][ni][2], acc[mi][ni][3])};
        *(LAS u32x2*)(smem + (wm * 64 + mi * 16 + fr) * OPITCH + (wn * 112 + ni * 16 + fh * 4) * 2) = o;
      }
  }
  WAIT_L0();
  BAR();
#pragma unroll
  for (int r = 0; r < 7; ++r) {
    const int c = r * 512 + tid;
    const int row = c / 28, ch = c - row * 28;
    const u32x4 v = *(const LAS u32x4*)(smem + row * OPITCH + ch * 16);
    __builtin_nontemporal_store(v, (u32x4*)((char*)out + (size_t)(m0 + row) * (N_ * 2) + n0 * 2 + ch * 16));
  }
  STAMP(7);
}

extern "C" void kernel_launch(void* const* d_in, const int* in_sizes, int n_in, void* d_out, int out_size, void* d_ws, size_t ws_size,
                              hipStream_t stream) {
  const u16* x = (const u16*)d_in[0];
  const int* qw = (const int*)d_in[1];
  const u16* cb = (const u16*)d_in[2];
  const u16* am = (const u16*)d_in[3];
  (void)hipFuncSetAttribute((const void*)k_fused, hipFuncAttributeMaxDynamicSharedMemorySize, LDS_B);
  k_fused<<<256, 512, LDS_B, stream>>>(x, qw, cb, am, (char*)d_ws, (u16*)d_out);
}
```

```cpp
#define SPIN 6000ull
#include <hip/hip_runtime.h>
#include <stdint.h>
typedef unsigned short u16;
typedef __attribute__((ext_vector_type(8))) __bf16 bf16x8;
typedef __attribute__((ext_vector_type(2))) __bf16 bf16x2;
typedef float f32x4 __attribute__((ext_vector_type(4)));
typedef float f32x2 __attribute__((ext_vector_type(2)));
typedef int i32x4 __attribute__((ext_vector_type(4)));
typedef unsigned u32x4 __attribute__((ext_vector_type(4)));
typedef unsigned u32x2 __attribute__((ext_vector_type(2)));
#define LAS __attribute__((address_space(3)))

constexpr int M_ = 2048, N_ = 3584, K_ = 1024;
constexpr int BM = 128, BN = 224;
constexpr int XT_B = BM * 64 * 2;
constexpr int WT_B = BN * 64 * 2;
constexpr int SLOT_B = XT_B + WT_B;
constexpr int NSLOT = 3;
constexpr int KB_B = BN * 16;
constexpr int J_B = (K_ / 8) * KB_B;
constexpr int W_TOTAL = (N_ / BN) * J_B;
constexpr int FLAG_OFF = W_TOTAL;
constexpr int EPOCH_OFF = FLAG_OFF + 128 * 128;
constexpr int SCR_OFF = NSLOT * SLOT_B;
constexpr int SCR_W = 3712;
constexpr int DUMMY_OFF = SCR_OFF + 4 * SCR_W;
constexpr int LDS_B = DUMMY_OFF + 256;
constexpr int OPITCH = 464;

#define STAG 10

__device__ __forceinline__ unsigned pk_bf16(float a, float b) {
  f32x2 v = {a, b};
  bf16x2 r = __builtin_convertvector(v, bf16x2);
  return __builtin_bit_cast(unsigned, r);
}
__device__ __forceinline__ float bf_lo(unsigned u) { return __uint_as_float(u << 16); }
__device__ __forceinline__ float bf_hi(unsigned u) { return __uint_as_float(u & 0xffff0000u); }

#define WAIT_V(n) asm volatile("s_waitcnt vmcnt(%0)" ::"n"(n) : "memory")
#define WAIT_L0() __builtin_amdgcn_s_waitcnt(0xC07F)
#define BAR() do { asm volatile("" ::: "memory"); __builtin_amdgcn_s_barrier(); asm volatile("" ::: "memory"); } while (0)

#define STAMP(i)
#define DIAG_ARG

__global__ __launch_bounds__(512, 2) void k_fused(const u16* __restrict__ x, const int* __restrict__ qw, const u16* __restrict__ cb,
                                                  const u16* __restrict__ am, char* __restrict__ ws, u16* __restrict__ out DIAG_ARG) {
  extern __shared__ __attribute__((aligned(16))) char smem_g[];
  LAS char* smem = (LAS char*)smem_g;
  const int tid = threadIdx.x, lane = tid & 63;
  const int w = __builtin_amdgcn_readfirstlane(tid >> 6);
  const int b = blockIdx.x, xcd = b & 7, bi = b >> 3;
  const int mt = (xcd & 1) * 8 + (bi & 7), nt = (xcd >> 1) * 4 + (bi >> 3);
  const int m0 = mt * BM, n0 = nt * BN;
  const unsigned long long T00 = __builtin_amdgcn_s_memrealtime();
  STAMP(0);
  u32x2 E;
  {
    const char* pe = ws + EPOCH_OFF + nt * 128;
    asm volatile("global_load_dwordx2 %0, %1, off sc1" : "=v"(E) : "v"(pe) : "memory");
  }
  const __amdgpu_buffer_rsrc_t rw = __builtin_amdgcn_make_buffer_rsrc((void*)ws, 0, EPOCH_OFF + 16 * 128, 0x00020000);

  if (w >= 4) {
    const int lw = w - 4;
    const __amdgpu_buffer_rsrc_t rx = __builtin_amdgcn_make_buffer_rsrc((void*)x, 0, M_ * K_ * 2, 0x00020000);
    const int p = lane & 7, rl = lane >> 3, h = lane >> 4;
    int vx[2];
#pragma unroll
    for (int par = 0; par < 2; ++par) vx[par] = rl * (K_ * 2) + (p ^ (4 * par + h)) * 16;
    const int vw = lane * 16;
    const int sx0 = (m0 + lw * 32) * (K_ * 2);
    const int sw0 = nt * J_B + lw * 7 * 1024;
    auto issue_x = [&](int kt, int slot) {
      LAS char* base = smem + slot * SLOT_B;
#pragma unroll
      for (int i = 0; i < 4; ++i)
        __builtin_amdgcn_raw_ptr_buffer_load_lds(rx, (LAS void*)(base + (lw * 4 + i) * 1024), 16, vx[i & 1], sx0 + i * 8 * (K_ * 2) + kt * 128, 0, 0);
    };
    auto issue_w = [&](int kt, int slot) {
      LAS char* base = smem + slot * SLOT_B;
#pragma unroll
      for (int i = 0; i < 7; ++i)
        __builtin_amdgcn_raw_ptr_buffer_load_lds(rw, (LAS void*)(base + XT_B + (lw * 7 + i) * 1024), 16, vw, sw0 + i * 1024 + kt * (8 * KB_B), 0, 16);
    };
    const char* pf = ws + FLAG_OFF + nt * 8 * 128 + (lane & 15) * 8;
    u32x2 F;
#define POLL_ISSUE(g) do { const char* pp_ = pf + (g) * 128; asm volatile("global_load_dwordx2 %0, %1, off sc1" : "=v"(F) : "v"(pp_) : "memory"); } while (0)
#define POLL_OK() (__builtin_amdgcn_ballot_w64(F.x == tag.x && F.y == tag.y) == ~0ull)
#define PIN_F() asm volatile("" : "+v"(F))
    issue_x(0, 0);
    issue_x(1, 1);
    {
      const int vp = ((nt & 3) * 32 + (lane & 31)) * (K_ * 2) + (lane >> 5) * 128;
#pragma unroll
      for (int i = 0; i < 2; ++i) {
        const int ii = lw * 2 + i;
        if (ii < 7)
          __builtin_amdgcn_raw_ptr_buffer_load_lds(rx, (LAS void*)(smem + DUMMY_OFF), 4, vp, m0 * (K_ * 2) + (2 + 2 * ii) * 128, 0, 0);
      }
    }
    WAIT_V(0);
    asm volatile("" : "+v"(E));
    u32x2 tag;
    tag.x = E.x + 1u;
    tag.y = E.y ^ 0x5bd1e995u;
    STAMP(1);
    auto spin = [&](int g) {
      const unsigned long long t0 = __builtin_amdgcn_s_memrealtime();
      for (;;) {
        POLL_ISSUE(g);
        WAIT_V(0);
        PIN_F();
        if (POLL_OK()) break;
        if (__builtin_amdgcn_s_memrealtime() - t0 > 2000000ull) break;
        __builtin_amdgcn_s_sleep(1);
      }
    };
    spin(0);
    STAMP(2);
    POLL_ISSUE(1);
    issue_w(0, 0);
    issue_w(1, 1);
    WAIT_V(7);
    PIN_F();
    bool ok = POLL_OK();
    STAMP(3);
    BAR();
    if (!ok) spin(1);
    int slot = 2;
#pragma unroll 1
    for (int t = 0; t < 14; t += 2) {
      issue_x(t + 2, slot);
      issue_w(t + 2, slot);
      slot = slot == 2 ? 0 : slot + 1;
      WAIT_V(11);
      BAR();
      const int g = (t + 4) >> 1;
      POLL_ISSUE(g < 8 ? g : 7);
      issue_x(t + 3, slot);
      issue_w(t + 3, slot);
      slot = slot == 2 ? 0 : slot + 1;
      WAIT_V(11);
      PIN_F();
      ok = POLL_OK();
      BAR();
      if (!ok) spin(g < 8 ? g : 7);
      if (g == 7 && mt == 0 && lw == 0 && lane == 0) {
        char* pe = ws + EPOCH_OFF + nt * 128;
        u32x2 ne = {E.x + 1u, E.y};
        asm volatile("global_store_dwordx2 %0, %1, off sc1" ::"v"(pe), "v"(ne) : "memory");
      }
    }
    WAIT_V(0);
    BAR();
    BAR();
  } else {
    const int wm = w & 1, wn = w >> 1;
    const int cbk = lane < 56 ? lane : 55;
    LAS char* scr = smem + SCR_OFF + w * SCR_W;
    u32x2 scA, scB;
    unsigned cbv;
    i32x4 q[8];
    {
      const u16* pa = am + (size_t)w * N_ + n0 + cbk * 4;
      const u16* pb = pa + 4 * N_;
      const u16* pc = cb + (lane & 15);
      asm volatile("global_load_dwordx2 %0, %1, off" : "=v"(scA) : "v"(pa) : "memory");
      asm volatile("global_load_dwordx2 %0, %1, off" : "=v"(scB) : "v"(pb) : "memory");
      asm volatile("global_load_ushort %0, %1, off" : "=v"(cbv) : "v"(pc) : "memory");
    }
    for (int i = 0; i < w; ++i) __builtin_amdgcn_s_sleep(STAG);
    const int* src = qw + (size_t)(w * 128 + mt * 8) * N_ + n0 + cbk * 4;
#define RAW_LOAD(base) _Pragma("unroll") for (int r = 0; r < 8; ++r) { const int* p_ = (base) + (size_t)r * N_; \
      asm volatile("global_load_dwordx4 %0, %1, off nt" : "=v"(q[r]) : "v"(p_) : "memory"); }
    RAW_LOAD(src);
    WAIT_V(8);
    asm volatile("" : "+v"(E), "+v"(scA), "+v"(scB), "+v"(cbv));
    u32x2 tag;
    tag.x = E.x + 1u;
    tag.y = E.y ^ 0x5bd1e995u;
    if (lane < 16) *(LAS float*)(scr + 3584 + lane * 4) = __uint_as_float(cbv << 16);
    const LAS float* lut = (const LAS float*)(scr + 3584);
    auto produce = [&](const u32x2 sc, int g) {
#pragma unroll
      for (int r = 0; r < 8; ++r) asm volatile("" : "+v"(q[r]));
      const float s[4] = {bf_lo(sc.x), bf_hi(sc.x), bf_lo(sc.y), bf_hi(sc.y)};
#pragma unroll
      for (int c = 0; c < 4; ++c) {
        u32x4 o;
#pragma unroll
        for (int pp = 0; pp < 4; ++pp) o[pp] = pk_bf16(lut[q[2 * pp][c]] * s[c], lut[q[2 * pp + 1][c]] * s[c]);
        if (lane < 56) *(LAS u32x4*)(scr + lane * 64 + c * 16) = o;
      }
      if (g < 4) STAMP(9);
      const int so = nt * J_B + (g * 16 + mt) * KB_B;
#pragma unroll
      for (int s2 = 0; s2 < 4; ++s2) {
        const u32x4 v = *(const LAS u32x4*)(scr + s2 * 1024 + lane * 16);
        const int vo = (s2 == 3 && lane >= 32) ? 0x7ffffff0 : lane * 16;
        __builtin_amdgcn_raw_buffer_store_b128(v, rw, vo, so + s2 * 1024, 16);
      }
    };
    WAIT_V(0);
    STAMP(1);
    produce(scA, w);
    STAMP(10);
    RAW_LOAD(src + (size_t)512 * N_);
    STAMP(11);
    WAIT_V(8);
    STAMP(2);
    char* pfl = ws + FLAG_OFF + nt * 8 * 128 + mt * 8;
    if (lane == 0) { char* pp_ = pfl + w * 128; asm volatile("global_store_dwordx2 %0, %1, off sc1" ::"v"(pp_), "v"(tag) : "memory"); }
    WAIT_V(1);
    STAMP(3);
    produce(scB, w + 4);
    const int fr = lane & 15, fh = lane >> 4;
    const int xa0 = (wm * 64 + fr) * 128 + (fh ^ (fr >> 1)) * 16;
    const int wa0 = XT_B + fh * KB_B + (wn * 112 + fr) * 16;
    f32x4 acc[4][7];
#pragma unroll
    for (int mi = 0; mi < 4; ++mi)
#pragma unroll
      for (int ni = 0; ni < 7; ++ni) acc[mi][ni] = (f32x4){0.f, 0.f, 0.f, 0.f};
    bf16x8 X0[4], W0[7], X1[4], W1[7];
#define RD(XF, WF, so, ks) do { \
    _Pragma("unroll") for (int ni = 0; ni < 7; ++ni) WF[ni] = *(const LAS bf16x8*)(smem + (so) + wa0 + (ks) * 4 * KB_B + ni * 256); \
    _Pragma("unroll") for (int mi = 0; mi < 4; ++mi) XF[mi] = *(const LAS bf16x8*)(smem + (so) + ((xa0 ^ ((ks) * 64))) + mi * 2048); } while (0)
#define MM(XF, WF) do { \
    _Pragma("unroll") for (int mi = 0; mi < 4; ++mi) _Pragma("unroll") for (int ni = 0; ni < 7; ++ni) \
      acc[mi][ni] = __builtin_amdgcn_mfma_f32_16x16x32_bf16(WF[ni], XF[mi], acc[mi][ni], 0, 0, 0); } while (0)
#define ILV() do { _Pragma("unroll") for (int g_ = 0; g_ < 11; ++g_) { \
      __builtin_amdgcn_sched_group_barrier(0x100, 1, 0); __builtin_amdgcn_sched_group_barrier(0x8, 2, 0); } \
      __builtin_amdgcn_sched_group_barrier(0x8, 6, 0); } while (0)
    STAMP(4);
    BAR();
    STAMP(5);
    int so = 0;
    RD(X0, W0, so, 0);
#pragma unroll 1
    for (int t = 0; t < 15; ++t) {
      WAIT_L0();
      __builtin_amdgcn_sched_barrier(0);
      RD(X1, W1, so, 1);
      MM(X0, W0);
      ILV();
      __builtin_amdgcn_sched_barrier(0);
      WAIT_L0();
      if (t == 1) {
        WAIT_V(0);
        if (lane == 0) { char* pp_ = pfl + (w + 4) * 128; asm volatile("global_store_dwordx2 %0, %1, off sc1" ::"v"(pp_), "v"(tag) : "memory"); }
      }
      BAR();
      __builtin_amdgcn_sched_barrier(0);
      so = so == 2 * SLOT_B ? 0 : so + SLOT_B;
      RD(X0, W0, so, 0);
      MM(X1, W1);
      ILV();
      __builtin_amdgcn_sched_barrier(0);
    }
    WAIT_L0();
    __builtin_amdgcn_sched_barrier(0);
    RD(X1, W1, so, 1);
    MM(X0, W0);
    ILV();
    __builtin_amdgcn_sched_barrier(0);
    WAIT_L0();
    BAR();
    MM(X1, W1);
    STAMP(6);
#pragma unroll
    for (int mi = 0; mi < 4; ++mi)
#pragma unroll
      for (int ni = 0; ni < 7; ++ni) {
        u32x2 o = {pk_bf16(acc[mi][ni][0], acc[mi][ni][1]), pk_bf16(acc[mi][ni][2], acc[mi][ni][3])};
        *(LAS u32x2*)(smem + (wm * 64 + mi * 16 + fr) * OPITCH + (wn * 112 + ni * 16 + fh * 4) * 2) = o;
      }
  }
  WAIT_L0();
  BAR();
#pragma unroll
  for (int r = 0; r < 7; ++r) {
    const int c = r * 512 + tid;
    const int row = c / 28, ch = c - row * 28;
    const u32x4 v = *(const LAS u32x4*)(smem + row * OPITCH + ch * 16);
    __builtin_nontemporal_store(v, (u32x4*)((char*)out + (size_t)(m0 + row) * (N_ * 2) + n0 * 2 + ch * 16));
  }
  STAMP(7);
  if (b == 0 && w == 0) { while (__builtin_amdgcn_s_memrealtime() - T00 < SPIN) __builtin_amdgcn_s_sleep(1); }
}

extern "C" void kernel_launch(void* const* d_in, const int* in_sizes, int n_in, void* d_out, int out_size, void* d_ws, size_t ws_size,
                              hipStream_t stream) {
  const u16* x = (const u16*)d_in[0];
  const int* qw = (const int*)d_in[1];
  const u16* cb = (const u16*)d_in[2];
  const u16* am = (const u16*)d_in[3];
  (void)hipFuncSetAttribute((const void*)k_fused, hipFuncAttributeMaxDynamicSharedMemorySize, LDS_B);
  k_fused<<<256, 512, LDS_B, stream>>>(x, qw, cb, am, (char*)d_ws, (u16*)d_out);
}
```

```cpp
#include <hip/hip_runtime.h>
#include <stdint.h>
typedef unsigned short u16;
typedef __attribute__((ext_vector_type(8))) __bf16 bf16x8;
typedef __attribute__((ext_vector_type(2))) __bf16 bf16x2;
typedef float f32x4 __attribute__((ext_vector_type(4)));
typedef float f32x2 __attribute__((ext_vector_type(2)));
typedef int i32x4 __attribute__((ext_vector_type(4)));
typedef unsigned u32x4 __attribute__((ext_vector_type(4)));
typedef unsigned u32x2 __attribute__((ext_vector_type(2)));
#define LAS __attribute__((address_space(3)))

constexpr int M_ = 2048, N_ = 3584, K_ = 1024;
constexpr int BM = 128, BN = 224;
constexpr int XT_B = BM * 64 * 2;
constexpr int WT_B = BN * 64 * 2;
constexpr int SLOT_B = XT_B + WT_B;
constexpr int NSLOT = 3;
constexpr int KB_B = BN * 16;
constexpr int J_B = (K_ / 8) * KB_B;
constexpr int W_TOTAL = (N_ / BN) * J_B;
constexpr int FLAG_OFF = W_TOTAL;
constexpr int EPOCH_OFF = FLAG_OFF + 128 * 128;
constexpr int SCR_OFF = NSLOT * SLOT_B;
constexpr int SCR_W = 3712;
constexpr int DUMMY_OFF = SCR_OFF + 4 * SCR_W;
constexpr int LDS_B = DUMMY_OFF + 256;
constexpr int OPITCH = 464;

#define STAG 10

__device__ __forceinline__ unsigned pk_bf16(float a, float b) {
  f32x2 v = {a, b};
  bf16x2 r = __builtin_convertvector(v, bf16x2);
  return __builtin_bit_cast(unsigned, r);
}
__device__ __forceinline__ float bf_lo(unsigned u) { return __uint_as_float(u << 16); }
__device__ __forceinline__ float bf_hi(unsigned u) { return __uint_as_float(u & 0xffff0000u); }

#define WAIT_V(n) asm volatile("s_waitcnt vmcnt(%0)" ::"n"(n) : "memory")
#define WAIT_L0() __builtin_amdgcn_s_waitcnt(0xC07F)
#define BAR() do { asm volatile("" ::: "memory"); __builtin_amdgcn_s_barrier(); asm volatile("" ::: "memory"); } while (0)

#define STAMP(i)
#define DIAG_ARG

__global__ __launch_bounds__(512, 2) void k_fused(const u16* __restrict__ x, const int* __restrict__ qw, const u16* __restrict__ cb,
                                                  const u16* __restrict__ am, char* __restrict__ ws, u16* __restrict__ out DIAG_ARG) {
  extern __shared__ __attribute__((aligned(16))) char smem_g[];
  LAS char* smem = (LAS char*)smem_g;
  const int tid = threadIdx.x, lane = tid & 63;
  const int w = __builtin_amdgcn_readfirstlane(tid >> 6);
  const int b = blockIdx.x, xcd = b & 7, bi = b >> 3;
  const int mt = (xcd & 1) * 8 + (bi & 7), nt = (xcd >> 1) * 4 + (bi >> 3);
  const int m0 = mt * BM, n0 = nt * BN;
  STAMP(0);
  u32x2 E;
  {
    const char* pe = ws + EPOCH_OFF + nt * 128;
    asm volatile("global_load_dwordx2 %0, %1, off sc1" : "=v"(E) : "v"(pe) : "memory");
  }
  const __amdgpu_buffer_rsrc_t rw = __builtin_amdgcn_make_buffer_rsrc((void*)ws, 0, EPOCH_OFF + 16 * 128, 0x00020000);

  if (w >= 4) {
    const int lw = w - 4;
    const __amdgpu_buffer_rsrc_t rx = __builtin_amdgcn_make_buffer_rsrc((void*)x, 0, M_ * K_ * 2, 0x00020000);
    const int p = lane & 7, rl = lane >> 3, h = lane >> 4;
    int vx[2];
#pragma unroll
    for (int par = 0; par < 2; ++par) vx[par] = rl * (K_ * 2) + (p ^ (4 * par + h)) * 16;
    const int vw = lane * 16;
    const int sx0 = (m0 + lw * 32) * (K_ * 2);
    const int sw0 = nt * J_B + lw * 7 * 1024;
    auto issue_x = [&](int kt, int slot) {
      LAS char* base = smem + slot * SLOT_B;
#pragma unroll
      for (int i = 0; i < 4; ++i)
        __builtin_amdgcn_raw_ptr_buffer_load_lds(rx, (LAS void*)(base + (lw * 4 + i) * 1024), 16, vx[i & 1], sx0 + i * 8 * (K_ * 2) + kt * 128, 0, 0);
    };
    auto issue_w = [&](int kt, int slot) {
      LAS char* base = smem + slot * SLOT_B;
#pragma unroll
      for (int i = 0; i < 7; ++i)
        __builtin_amdgcn_raw_ptr_buffer_load_lds(rw, (LAS void*)(base + XT_B + (lw * 7 + i) * 1024), 16, vw, sw0 + i * 1024 + kt * (8 * KB_B), 0, 16);
    };
    const char* pf = ws + FLAG_OFF + nt * 8 * 128 + (lane & 15) * 8;
    u32x2 F;
#define POLL_ISSUE(g) do { const char* pp_ = pf + (g) * 128; asm volatile("global_load_dwordx2 %0, %1, off sc1" : "=v"(F) : "v"(pp_) : "memory"); } while (0)
#define POLL_OK() (__builtin_amdgcn_ballot_w64(F.x == tag.x && F.y == tag.y) == ~0ull)
#define PIN_F() asm volatile("" : "+v"(F))
    issue_x(0, 0);
    issue_x(1, 1);
    {
      const int vp = ((nt & 3) * 32 + (lane & 31)) * (K_ * 2) + (lane >> 5) * 128;
#pragma unroll
      for (int i = 0; i < 2; ++i) {
        const int ii = lw * 2 + i;
        if (ii < 0)
          __builtin_amdgcn_raw_ptr_buffer_load_lds(rx, (LAS void*)(smem + DUMMY_OFF), 4, vp, m0 * (K_ * 2) + (2 + 2 * ii) * 128, 0, 0);
      }
    }
    WAIT_V(0);
    asm volatile("" : "+v"(E));
    u32x2 tag;
    tag.x = E.x + 1u;
    tag.y = E.y ^ 0x5bd1e995u;
    STAMP(1);
    auto spin = [&](int g) {
      const unsigned long long t0 = __builtin_amdgcn_s_memrealtime();
      for (;;) {
        POLL_ISSUE(g);
        WAIT_V(0);
        PIN_F();
        if (POLL_OK()) break;
        if (__builtin_amdgcn_s_memrealtime() - t0 > 2000000ull) break;
        __builtin_amdgcn_s_sleep(1);
      }
    };
    spin(0);
    STAMP(2);
    POLL_ISSUE(1);
    issue_w(0, 0);
    issue_w(1, 1);
    WAIT_V(7);
    PIN_F();
    bool ok = POLL_OK();
    STAMP(3);
    BAR();
    if (!ok) spin(1);
    int slot = 2;
#pragma unroll 1
    for (int t = 0; t < 14; t += 2) {
      issue_x(t + 2, slot);
      issue_w(t + 2, slot);
      slot = slot == 2 ? 0 : slot + 1;
      WAIT_V(11);
      BAR();
      const int g = (t + 4) >> 1;
      POLL_ISSUE(g < 8 ? g : 7);
      issue_x(t + 3, slot);
      issue_w(t + 3, slot);
      slot = slot == 2 ? 0 : slot + 1;
      WAIT_V(11);
      PIN_F();
      ok = POLL_OK();
      BAR();
      if (!ok) spin(g < 8 ? g : 7);
      if (g == 7 && mt == 0 && lw == 0 && lane == 0) {
        char* pe = ws + EPOCH_OFF + nt * 128;
        u32x2 ne = {E.x + 1u, E.y};
        asm volatile("global_store_dwordx2 %0, %1, off sc1" ::"v"(pe), "v"(ne) : "memory");
      }
    }
    WAIT_V(0);
    BAR();
    BAR();
  } else {
    const int wm = w & 1, wn = w >> 1;
    const int cbk = lane < 56 ? lane : 55;
    LAS char* scr = smem + SCR_OFF + w * SCR_W;
    u32x2 scA, scB;
    unsigned cbv;
    i32x4 q[8];
    {
      const u16* pa = am + (size_t)w * N_ + n0 + cbk * 4;
      const u16* pb = pa + 4 * N_;
      const u16* pc = cb + (lane & 15);
      asm volatile("global_load_dwordx2 %0, %1, off" : "=v"(scA) : "v"(pa) : "memory");
      asm volatile("global_load_dwordx2 %0, %1, off" : "=v"(scB) : "v"(pb) : "memory");
      asm volatile("global_load_ushort %0, %1, off" : "=v"(cbv) : "v"(pc) : "memory");
    }
    for (int i = 0; i < w; ++i) __builtin_amdgcn_s_sleep(STAG);
    const int* src = qw + (size_t)(w * 128 + mt * 8) * N_ + n0 + cbk * 4;
#define RAW_LOAD(base) _Pragma("unroll") for (int r = 0; r < 8; ++r) { const int* p_ = (base) + (size_t)r * N_; \
      asm volatile("global_load_dwordx4 %0, %1, off nt" : "=v"(q[r]) : "v"(p_) : "memory"); }
    RAW_LOAD(src);
    WAIT_V(8);
    asm volatile("" : "+v"(E), "+v"(scA), "+v"(scB), "+v"(cbv));
    u32x2 tag;
    tag.x = E.x + 1u;
    tag.y = E.y ^ 0x5bd1e995u;
    if (lane < 16) *(LAS float*)(scr + 3584 + lane * 4) = __uint_as_float(cbv << 16);
    const LAS float* lut = (const LAS float*)(scr + 3584);
    auto produce = [&](const u32x2 sc, int g) {
#pragma unroll
      for (int r = 0; r < 8; ++r) asm volatile("" : "+v"(q[r]));
      const float s[4] = {bf_lo(sc.x), bf_hi(sc.x), bf_lo(sc.y), bf_hi(sc.y)};
#pragma unroll
      for (int c = 0; c < 4; ++c) {
        u32x4 o;
#pragma unroll
        for (int pp = 0; pp < 4; ++pp) o[pp] = pk_bf16(lut[q[2 * pp][c]] * s[c], lut[q[2 * pp + 1][c]] * s[c]);
        if (lane < 56) *(LAS u32x4*)(scr + lane * 64 + c * 16) = o;
      }
      if (g < 4) STAMP(9);
      const int so = nt * J_B + (g * 16 + mt) * KB_B;
#pragma unroll
      for (int s2 = 0; s2 < 4; ++s2) {
        const u32x4 v = *(const LAS u32x4*)(scr + s2 * 1024 + lane * 16);
        const int vo = (s2 == 3 && lane >= 32) ? 0x7ffffff0 : lane * 16;
        __builtin_amdgcn_raw_buffer_store_b128(v, rw, vo, so + s2 * 1024, 16);
      }
    };
    WAIT_V(0);
    STAMP(1);
    produce(scA, w);
    STAMP(10);
    RAW_LOAD(src + (size_t)512 * N_);
    STAMP(11);
    WAIT_V(8);
    STAMP(2);
    char* pfl = ws + FLAG_OFF + nt * 8 * 128 + mt * 8;
    if (lane == 0) { char* pp_ = pfl + w * 128; asm volatile("global_store_dwordx2 %0, %1, off sc1" ::"v"(pp_), "v"(tag) : "memory"); }
    WAIT_V(1);
    STAMP(3);
    produce(scB, w + 4);
    const int fr = lane & 15, fh = lane >> 4;
    const int xa0 = (wm * 64 + fr) * 128 + (fh ^ (fr >> 1)) * 16;
    const int wa0 = XT_B + fh * KB_B + (wn * 112 + fr) * 16;
    f32x4 acc[4][7];
#pragma unroll
    for (int mi = 0; mi < 4; ++mi)
#pragma unroll
      for (int ni = 0; ni < 7; ++ni) acc[mi][ni] = (f32x4){0.f, 0.f, 0.f, 0.f};
    bf16x8 X0[4], W0[7], X1[4], W1[7];
#define RD(XF, WF, so, ks) do { \
    _Pragma("unroll") for (int ni = 0; ni < 7; ++ni) WF[ni] = *(const LAS bf16x8*)(smem + (so) + wa0 + (ks) * 4 * KB_B + ni * 256); \
    _Pragma("unroll") for (int mi = 0; mi < 4; ++mi) XF[mi] = *(const LAS bf16x8*)(smem + (so) + ((xa0 ^ ((ks) * 64))) + mi * 2048); } while (0)
#define MM(XF, WF) do { \
    _Pragma("unroll") for (int mi = 0; mi < 4; ++mi) _Pragma("unroll") for (int ni = 0; ni < 7; ++ni) \
      acc[mi][ni] = __builtin_amdgcn_mfma_f32_16x16x32_bf16(WF[ni], XF[mi], acc[mi][ni], 0, 0, 0); } while (0)
#define ILV() do { _Pragma("unroll") for (int g_ = 0; g_ < 11; ++g_) { \
      __builtin_amdgcn_sched_group_barrier(0x100, 1, 0); __builtin_amdgcn_sched_group_barrier(0x8, 2, 0); } \
      __builtin_amdgcn_sched_group_barrier(0x8, 6, 0); } while (0)
    STAMP(4);
    BAR();
    STAMP(5);
    int so = 0;
    RD(X0, W0, so, 0);
#pragma unroll 1
    for (int t = 0; t < 15; ++t) {
      WAIT_L0();
      __builtin_amdgcn_sched_barrier(0);
      RD(X1, W1, so, 1);
      MM(X0, W0);
      ILV();
      __builtin_amdgcn_sched_barrier(0);
      WAIT_L0();
      if (t == 1) {
        WAIT_V(0);
        if (lane == 0) { char* pp_ = pfl + (w + 4) * 128; asm volatile("global_store_dwordx2 %0, %1, off sc1" ::"v"(pp_), "v"(tag) : "memory"); }
      }
      BAR();
      __builtin_amdgcn_sched_barrier(0);
      so = so == 2 * SLOT_B ? 0 : so + SLOT_B;
      RD(X0, W0, so, 0);
      MM(X1, W1);
      ILV();
      __builtin_amdgcn_sched_barrier(0);
    }
    WAIT_L0();
    __builtin_amdgcn_sched_barrier(0);
    RD(X1, W1, so, 1);
    MM(X0, W0);
    ILV();
    __builtin_amdgcn_sched_barrier(0);
    WAIT_L0();
    BAR();
    MM(X1, W1);
    STAMP(6);
#pragma unroll
    for (int mi = 0; mi < 4; ++mi)
#pragma unroll
      for (int ni = 0; ni < 7; ++ni) {
        u32x2 o = {pk_bf16(acc[mi][ni][0], acc[mi][ni][1]), pk_bf16(acc[mi][ni][2], acc[mi][ni][3])};
        *(LAS u32x2*)(smem + (wm * 64 + mi * 16 + fr) * OPITCH + (wn * 112 + ni * 16 + fh * 4) * 2) = o;
      }
  }
  WAIT_L0();
  BAR();
#pragma unroll
  for (int r = 0; r < 7; ++r) {
    const int c = r * 512 + tid;
    const int row = c / 28, ch = c - row * 28;
    const u32x4 v = *(const LAS u32x4*)(smem + row * OPITCH + ch * 16);
    __builtin_nontemporal_store(v, (u32x4*)((char*)out + (size_t)(m0 + row) * (N_ * 2) + n0 * 2 + ch * 16));
  }
  STAMP(7);
}

extern "C" void kernel_launch(void* const* d_in, const int* in_sizes, int n_in, void* d_out, int out_size, void* d_ws, size_t ws_size,
                              hipStream_t stream) {
  const u16* x = (const u16*)d_in[0];
  const int* qw = (const int*)d_in[1];
  const u16* cb = (const u16*)d_in[2];
  const u16* am = (const u16*)d_in[3];
  (void)hipFuncSetAttribute((const void*)k_fused, hipFuncAttributeMaxDynamicSharedMemorySize, LDS_B);
  k_fused<<<256, 512, LDS_B, stream>>>(x, qw, cb, am, (char*)d_ws, (u16*)d_out);
}
```

```cpp
#include <hip/hip_runtime.h>
#include <stdint.h>
typedef unsigned short u16;
typedef __attribute__((ext_vector_type(8))) __bf16 bf16x8;
typedef __attribute__((ext_vector_type(2))) __bf16 bf16x2;
typedef float f32x4 __attribute__((ext_vector_type(4)));
typedef float f32x2 __attribute__((ext_vector_type(2)));
typedef int i32x4 __attribute__((ext_vector_type(4)));
typedef unsigned u32x4 __attribute__((ext_vector_type(4)));
typedef unsigned u32x2 __attribute__((ext_vector_type(2)));
#define LAS __attribute__((address_space(3)))

constexpr int M_ = 2048, N_ = 3584, K_ = 1024;
constexpr int BM = 128, BN = 224;
constexpr int XT_B = BM * 64 * 2;
constexpr int WT_B = BN * 64 * 2;
constexpr int SLOT_B = XT_B + WT_B;
constexpr int NSLOT = 3;
constexpr int KB_B = BN * 16;
constexpr int J_B = (K_ / 8) * KB_B;
constexpr int W_TOTAL = (N_ / BN) * J_B;
constexpr int FLAG_OFF = W_TOTAL;
constexpr int EPOCH_OFF = FLAG_OFF + 128 * 128;
constexpr int SCR_OFF = NSLOT * SLOT_B;
constexpr int SCR_W = 3712;
constexpr int DUMMY_OFF = SCR_OFF + 6 * SCR_W;
constexpr int SIG_OFF = DUMMY_OFF + 256;
constexpr int LDS_B = SIG_OFF + 16;
constexpr int OPITCH = 464;

#define T_G1 60
#define T_G2 120
#define T_G3 170
#define T_XWA 25
#define T_XWB 120
#define T_POLL 200
#define E2 2
#define E2_SLEEP 12
#define SELFPF 0

__device__ __forceinline__ unsigned pk_bf16(float a, float b) {
  f32x2 v = {a, b};
  bf16x2 r = __builtin_convertvector(v, bf16x2);
  return __builtin_bit_cast(unsigned, r);
}
__device__ __forceinline__ float bf_lo(unsigned u) { return __uint_as_float(u << 16); }
__device__ __forceinline__ float bf_hi(unsigned u) { return __uint_as_float(u & 0xffff0000u); }

#define WAIT_V(n) asm volatile("s_waitcnt vmcnt(%0)" ::"n"(n) : "memory")
#define WAIT_L0() __builtin_amdgcn_s_waitcnt(0xC07F)
#define BAR() do { asm volatile("" ::: "memory"); __builtin_amdgcn_s_barrier(); asm volatile("" ::: "memory"); } while (0)

#define STAMP(i)
#define DIAG_ARG

__device__ __forceinline__ void wait_until(unsigned long long t0, unsigned ticks) {
  while (__builtin_amdgcn_s_memrealtime() - t0 < ticks) __builtin_amdgcn_s_sleep(1);
}

__global__ __launch_bounds__(512, 2) void k_fused(const u16* __restrict__ x, const int* __restrict__ qw, const u16* __restrict__ cb,
                                                  const u16* __restrict__ am, char* __restrict__ ws, u16* __restrict__ out DIAG_ARG) {
  extern __shared__ __attribute__((aligned(16))) char smem_g[];
  LAS char* smem = (LAS char*)smem_g;
  const unsigned long long T0 = __builtin_amdgcn_s_memrealtime();
  const int tid = threadIdx.x, lane = tid & 63;
  const int w = __builtin_amdgcn_readfirstlane(tid >> 6);
  const int b = blockIdx.x, xcd = b & 7, bi = b >> 3;
#define MAP 0
  const int mt = MAP ? (bi & 15) : (xcd & 1) * 8 + (bi & 7), nt = MAP ? xcd * 2 + (bi >> 4) : (xcd >> 1) * 4 + (bi >> 3);
  const int m0 = mt * BM, n0 = nt * BN;
  STAMP(0);
  const __amdgpu_buffer_rsrc_t rw = __builtin_amdgcn_make_buffer_rsrc((void*)ws, 0, EPOCH_OFF + 16 * 128, 0x00020000);
  const char* pe = ws + EPOCH_OFF + nt * 128;
  char* pfl = ws + FLAG_OFF + nt * 8 * 128 + mt * 8;

  const int cbk = lane < 56 ? lane : 55;
  const int* qsrc = qw + (size_t)(mt * 8) * N_ + n0 + cbk * 4;
  i32x4 q[8];
#define RAW_LOAD(g) do { const int* s_ = qsrc + (size_t)(g) * 128 * N_; _Pragma("unroll") for (int r = 0; r < 8; ++r) { const int* p_ = s_ + (size_t)r * N_; \
      asm volatile("global_load_dwordx4 %0, %1, off nt" : "=v"(q[r]) : "v"(p_) : "memory"); } } while (0)
#define SCALE_LOAD(dst, g) do { const u16* p_ = am + (size_t)(g) * N_ + n0 + cbk * 4; asm volatile("global_load_dwordx2 %0, %1, off" : "=v"(dst) : "v"(p_) : "memory"); } while (0)
#define E_LOAD() asm volatile("global_load_dwordx2 %0, %1, off sc1" : "=v"(E) : "v"(pe) : "memory")
#define FLAG_STORE(g) do { char* p_ = pfl + (g) * 128; asm volatile("global_store_dwordx2 %0, %1, off sc1" ::"v"(p_), "v"(tag) : "memory"); } while (0)
  auto dequant = [&](LAS char* scr, const u32x2 sc, int c0, int c1) {
#pragma unroll
    for (int r = 0; r < 8; ++r) asm volatile("" : "+v"(q[r]));
    const LAS float* lut = (const LAS float*)(scr + 3584);
    const float s[4] = {bf_lo(sc.x), bf_hi(sc.x), bf_lo(sc.y), bf_hi(sc.y)};
#pragma unroll
    for (int c = c0; c < c1; ++c) {
      u32x4 o;
#pragma unroll
      for (int pp = 0; pp < 4; ++pp) o[pp] = pk_bf16(lut[q[2 * pp][c]] * s[c], lut[q[2 * pp + 1][c]] * s[c]);
      if (lane < 56) *(LAS u32x4*)(scr + lane * 64 + c * 16) = o;
    }
  };
  auto publish = [&](LAS char* scr, int g) {
    const int so = nt * J_B + (g * 16 + mt) * KB_B;
#pragma unroll
    for (int s2 = 0; s2 < 4; ++s2) {
      const u32x4 v = *(const LAS u32x4*)(scr + s2 * 1024 + lane * 16);
      const int vo = ((s2 == 3 && lane >= 32) ? 0x7f000000 : lane * 16) + so + s2 * 1024;
      __builtin_amdgcn_raw_buffer_store_b128(v, rw, vo, 0, 16);
    }
  };
#define SELF_PF(g) if (SELFPF) __builtin_amdgcn_raw_ptr_buffer_load_lds(rw, (LAS void*)(smem + DUMMY_OFF), 4, lane < 28 ? lane * 128 : 0x7ffffff0, nt * J_B + ((g) * 16 + mt) * KB_B, 0, 16)
  u32x2 E, tag;
  unsigned cbv;

  if (w >= 6) {
    const int pw = w - 6;
    __builtin_amdgcn_s_setprio(3);
    LAS char* scr = smem + SCR_OFF + (4 + pw) * SCR_W;
    u32x2 sA, sB;
    E_LOAD();
    { const u16* pc = cb + (lane & 15); asm volatile("global_load_ushort %0, %1, off" : "=v"(cbv) : "v"(pc) : "memory"); }
    SCALE_LOAD(sA, 4 + pw);
    SCALE_LOAD(sB, 6 + pw);
    WAIT_V(0);
    asm volatile("" : "+v"(E), "+v"(cbv), "+v"(sA), "+v"(sB));
    tag.x = E.x + 1u;
    tag.y = E.y ^ 0x5bd1e995u;
    if (lane < 16) *(LAS float*)(scr + 3584 + lane * 4) = __uint_as_float(cbv << 16);
    {
      const unsigned long long t0 = __builtin_amdgcn_s_memrealtime();
      while (*(volatile LAS unsigned*)(smem + SIG_OFF) != 0x600dbeefu && __builtin_amdgcn_s_memrealtime() - t0 < 2000000ull) __builtin_amdgcn_s_sleep(1);
    }
#define PBAR(t) do { STAMP(9 + (t)); BAR(); } while (0)
    RAW_LOAD(4 + pw);
    STAMP(3);
    BAR();
    WAIT_V(0);
    dequant(scr, sA, 0, 2);
    PBAR(0);
    dequant(scr, sA, 2, 4);
    publish(scr, 4 + pw);
    PBAR(1);
    __builtin_amdgcn_s_sleep(E2_SLEEP);
    RAW_LOAD(6 + pw);
    PBAR(2);
    WAIT_V(8);
    FLAG_STORE(4 + pw);
    PBAR(3);
    WAIT_V(1);
    dequant(scr, sB, 0, 2);
    PBAR(4);
    dequant(scr, sB, 2, 4);
    publish(scr, 6 + pw);
    PBAR(5);
    PBAR(6);
    WAIT_V(0);
    FLAG_STORE(6 + pw);
    PBAR(7);
    PBAR(8); PBAR(9); PBAR(10); PBAR(11); PBAR(12); PBAR(13); PBAR(14); PBAR(15);
  } else if (w >= 4) {
    const int lw = w - 4;
    const __amdgpu_buffer_rsrc_t rx = __builtin_amdgcn_make_buffer_rsrc((void*)x, 0, M_ * K_ * 2, 0x00020000);
    const int p = lane & 7, rl = lane >> 3, h = lane >> 4;
    int vx[2];
#pragma unroll
    for (int par = 0; par < 2; ++par) vx[par] = rl * (K_ * 2) + (p ^ (4 * par + h)) * 16;
    const int vw = lane * 16;
    const int sx0 = (m0 + lw * 64) * (K_ * 2);
    const int sw0 = nt * J_B + lw * 14 * 1024;
    auto issue_xwarm = [&](int piece) {
      __builtin_amdgcn_raw_ptr_buffer_load_lds(rx, (LAS void*)(smem + DUMMY_OFF), 4, lane < 32 ? (lane >> 2) * (K_ * 2) + (lane & 3) * 128 : 0x7ffffff0,
                                               b * 8 * (K_ * 2) + piece * 512, 0, 0);
    };
    auto issue_x = [&](int kt, int slot) {
      LAS char* base = smem + slot * SLOT_B;
#pragma unroll
      for (int i = 0; i < 8; ++i)
        __builtin_amdgcn_raw_ptr_buffer_load_lds(rx, (LAS void*)(base + (lw * 8 + i) * 1024), 16, vx[i & 1], sx0 + i * 8 * (K_ * 2) + kt * 128, 0, 0);
    };
    auto issue_w = [&](int kt, int slot) {
      LAS char* base = smem + slot * SLOT_B;
#pragma unroll
      for (int i = 0; i < 14; ++i)
        __builtin_amdgcn_raw_ptr_buffer_load_lds(rw, (LAS void*)(base + XT_B + (lw * 14 + i) * 1024), 16, vw, sw0 + i * 1024 + kt * (8 * KB_B), 0, 16);
    };
    const char* pf = ws + FLAG_OFF + nt * 8 * 128 + (lane & 15) * 8;
    u32x2 Fa, Fb, Fs;
#define POLL_ISSUE(F, g) do { const char* pp_ = pf + (g) * 128; asm volatile("global_load_dwordx2 %0, %1, off sc1" : "=v"(F) : "v"(pp_) : "memory"); } while (0)
#define POLL_OK(F) (__builtin_amdgcn_ballot_w64(F.x == tag.x && F.y == tag.y) == ~0ull)
#define PIN(F) asm volatile("" : "+v"(F))
    if (lw == 0 && lane == 0) *(volatile LAS unsigned*)(smem + SIG_OFF) = 0u;
    E_LOAD();
    wait_until(T0, lw ? T_XWB : T_XWA);
    issue_xwarm(lw);
    WAIT_V(1);
    asm volatile("" : "+v"(E));
    tag.x = E.x + 1u;
    tag.y = E.y ^ 0x5bd1e995u;
    wait_until(T0, T_POLL);
    STAMP(1);
    auto spin = [&](int g) {
      const unsigned long long t0 = __builtin_amdgcn_s_memrealtime();
      for (;;) {
        POLL_ISSUE(Fs, g);
        WAIT_V(0);
        PIN(Fs);
        if (POLL_OK(Fs)) break;
        if (__builtin_amdgcn_s_memrealtime() - t0 > 2000000ull) break;
        __builtin_amdgcn_s_sleep(1);
      }
    };
    spin(0);
    STAMP(2);
    issue_x(0, 0);
    issue_w(0, 0);
    issue_x(1, 1);
    issue_w(1, 1);
    POLL_ISSUE(Fa, 1);
    POLL_ISSUE(Fb, 2);
    issue_xwarm(2 + lw);
    if (lw == 0 && lane == 0) *(volatile LAS unsigned*)(smem + SIG_OFF) = 0x600dbeefu;
    WAIT_V(25);
    STAMP(3);
    BAR();
    WAIT_V(1);
    PIN(Fa); PIN(Fb);
    if (!POLL_OK(Fa)) spin(1);
    int slot = 2;
#define TRIP(j, FNEW, FOLD) do { \
      issue_x(2 * (j) + 2, slot); \
      issue_w(2 * (j) + 2, slot); \
      slot = slot == 2 ? 0 : slot + 1; \
      WAIT_V(23); \
      STAMP(9 + 2 * (j)); \
      BAR(); \
      issue_x(2 * (j) + 3, slot); \
      issue_w(2 * (j) + 3, slot); \
      slot = slot == 2 ? 0 : slot + 1; \
      POLL_ISSUE(FNEW, (j) + 3 < 8 ? (j) + 3 : 7); \
      WAIT_V(23); \
      PIN(FOLD); \
      const bool ok_ = POLL_OK(FOLD); \
      STAMP(10 + 2 * (j)); \
      BAR(); \
      if (!ok_ && (j) + 2 < 8) spin((j) + 2); \
    } while (0)
    TRIP(0, Fa, Fb);
#pragma unroll 1
    for (int j = 1; j < 7; j += 2) {
      TRIP(j, Fb, Fa);
      if (j == 5 && mt == 0 && lw == 0 && lane == 0) {
        const u32x2 ne = {E.x + 1u, E.y};
        asm volatile("global_store_dwordx2 %0, %1, off sc1" ::"v"(pe), "v"(ne) : "memory");
      }
      TRIP(j + 1, Fa, Fb);
    }
    WAIT_V(0);
    BAR();
    BAR();
  } else {
    const int wm = w & 1, wn = w >> 1;
    {
      LAS char* scr = smem + SCR_OFF + w * SCR_W;
      u32x2 sA;
      E_LOAD();
      { const u16* pc = cb + (lane & 15); asm volatile("global_load_ushort %0, %1, off" : "=v"(cbv) : "v"(pc) : "memory"); }
      SCALE_LOAD(sA, w);
      if (w) wait_until(T0, w == 1 ? T_G1 : w == 2 ? T_G2 : T_G3);
      RAW_LOAD(w);
      WAIT_V(8);
      asm volatile("" : "+v"(E), "+v"(cbv), "+v"(sA));
      tag.x = E.x + 1u;
      tag.y = E.y ^ 0x5bd1e995u;
      if (lane < 16) *(LAS float*)(scr + 3584 + lane * 4) = __uint_as_float(cbv << 16);
      WAIT_V(0);
      STAMP(1);
      dequant(scr, sA, 0, 4);
      publish(scr, w);
      STAMP(2);
      WAIT_V(0);
      FLAG_STORE(w);
      SELF_PF(w);
      STAMP(3);
    }
    const int fr = lane & 15, fh = lane >> 4;
    const int xa0 = (wm * 64 + fr) * 128 + (fh ^ (fr >> 1)) * 16;
    const int wa0 = XT_B + fh * KB_B + (wn * 112 + fr) * 16;
    f32x4 acc[4][7];
#pragma unroll
    for (int mi = 0; mi < 4; ++mi)
#pragma unroll
      for (int ni = 0; ni < 7; ++ni) acc[mi][ni] = (f32x4){0.f, 0.f, 0.f, 0.f};
    bf16x8 X0[4], W0[7], X1[4], W1[7];
#define RD(XF, WF, so, ks) do { \
    _Pragma("unroll") for (int ni = 0; ni < 7; ++ni) WF[ni] = *(const LAS bf16x8*)(smem + (so) + wa0 + (ks) * 4 * KB_B + ni * 256); \
    _Pragma("unroll") for (int mi = 0; mi < 4; ++mi) XF[mi] = *(const LAS bf16x8*)(smem + (so) + ((xa0 ^ ((ks) * 64))) + mi * 2048); } while (0)
#define MM(XF, WF) do { \
    _Pragma("unroll") for (int mi = 0; mi < 4; ++mi) _Pragma("unroll") for (int ni = 0; ni < 7; ++ni) \
      acc[mi][ni] = __builtin_amdgcn_mfma_f32_16x16x32_bf16(WF[ni], XF[mi], acc[mi][ni], 0, 0, 0); } while (0)
#define ILV() do { _Pragma("unroll") for (int g_ = 0; g_ < 11; ++g_) { \
      __builtin_amdgcn_sched_group_barrier(0x100, 1, 0); __builtin_amdgcn_sched_group_barrier(0x8, 2, 0); } \
      __builtin_amdgcn_sched_group_barrier(0x8, 6, 0); } while (0)
    STAMP(4);
    BAR();
    STAMP(8);
    int so = 0;
    RD(X0, W0, so, 0);
#pragma unroll 1
    for (int t = 0; t < 15; ++t) {
      WAIT_L0();
      __builtin_amdgcn_sched_barrier(0);
      RD(X1, W1, so, 1);
      MM(X0, W0);
      ILV();
      __builtin_amdgcn_sched_barrier(0);
      WAIT_L0();
      STAMP(9 + t);
      BAR();
      __builtin_amdgcn_sched_barrier(0);
      so = so == 2 * SLOT_B ? 0 : so + SLOT_B;
      RD(X0, W0, so, 0);
      MM(X1, W1);
      ILV();
      __builtin_amdgcn_sched_barrier(0);
    }
    WAIT_L0();
    __builtin_amdgcn_sched_barrier(0);
    RD(X1, W1, so, 1);
    MM(X0, W0);
    ILV();
    __builtin_amdgcn_sched_barrier(0);
    WAIT_L0();
    STAMP(24);
    BAR();
    MM(X1, W1);
    STAMP(5);
#pragma unroll
    for (int mi = 0; mi < 4; ++mi)
#pragma unroll
      for (int ni = 0; ni < 7; ++ni) {
        u32x2 o = {pk_bf16(acc[mi][ni][0], acc[mi][ni][1]), pk_bf16(acc[mi][ni][2], acc[mi][ni][3])};
        *(LAS u32x2*)(smem + (wm * 64 + mi * 16 + fr) * OPITCH + (wn * 112 + ni * 16 + fh * 4) * 2) = o;
      }
  }
  WAIT_L0();
  BAR();
#pragma unroll
  for (int r = 0; r < 7; ++r) {
    const int c = r * 512 + tid;
    const int row = c / 28, ch = c - row * 28;
    const u32x4 v = *(const LAS u32x4*)(smem + row * OPITCH + ch * 16);
    __builtin_nontemporal_store(v, (u32x4*)((char*)out + (size_t)(m0 + row) * (N_ * 2) + n0 * 2 + ch * 16));
  }
  STAMP(6);
}

extern "C" void kernel_launch(void* const* d_in, const int* in_sizes, int n_in, void* d_out, int out_size, void* d_ws, size_t ws_size,
                              hipStream_t stream) {
  const u16* x = (const u16*)d_in[0];
  const int* qw = (const int*)d_in[1];
  const u16* cb = (const u16*)d_in[2];
  const u16* am = (const u16*)d_in[3];
  (void)hipFuncSetAttribute((const void*)k_fused, hipFuncAttributeMaxDynamicSharedMemorySize, LDS_B);
  k_fused<<<256, 512, LDS_B, stream>>>(x, qw, cb, am, (char*)d_ws, (u16*)d_out);
}
```

```cpp
#define T_G1 0
#define T_G2 0
#define T_G3 0
#define T_G4 0
#define T_G5 0
#define T_XWA 0
#define T_XWB 0
#define T_POLL 0
#include <hip/hip_runtime.h>
#include <stdint.h>
typedef unsigned short u16;
typedef __attribute__((ext_vector_type(8))) __bf16 bf16x8;
typedef __attribute__((ext_vector_type(2))) __bf16 bf16x2;
typedef float f32x4 __attribute__((ext_vector_type(4)));
typedef float f32x2 __attribute__((ext_vector_type(2)));
typedef int i32x4 __attribute__((ext_vector_type(4)));
typedef unsigned u32x4 __attribute__((ext_vector_type(4)));
typedef unsigned u32x2 __attribute__((ext_vector_type(2)));
#define LAS __attribute__((address_space(3)))

constexpr int M_ = 2048, N_ = 3584, K_ = 1024;
constexpr int BM = 128, BN = 224;
constexpr int XT_B = BM * 64 * 2;
constexpr int WT_B = BN * 64 * 2;
constexpr int SLOT_B = XT_B + WT_B;
constexpr int NSLOT = 3;
constexpr int KB_B = BN * 16;
constexpr int J_B = (K_ / 8) * KB_B;
constexpr int W_TOTAL = (N_ / BN) * J_B;
constexpr int FLAG_OFF = W_TOTAL;
constexpr int EPOCH_OFF = FLAG_OFF + 128 * 128;
constexpr int SCR_OFF = NSLOT * SLOT_B;
constexpr int SCR_W = 3712;
constexpr int DUMMY_OFF = SCR_OFF + 6 * SCR_W;
constexpr int SIG_OFF = DUMMY_OFF + 256;
constexpr int LDS_B = SIG_OFF + 16;
constexpr int OPITCH = 464;

#define T_G6 0
#define T_G7 0
#define SELFPF 0

__device__ __forceinline__ unsigned pk_bf16(float a, float b) {
  f32x2 v = {a, b};
  bf16x2 r = __builtin_convertvector(v, bf16x2);
  return __builtin_bit_cast(unsigned, r);
}
__device__ __forceinline__ float bf_lo(unsigned u) { return __uint_as_float(u << 16); }
__device__ __forceinline__ float bf_hi(unsigned u) { return __uint_as_float(u & 0xffff0000u); }

#define WAIT_V(n) asm volatile("s_waitcnt vmcnt(%0)" ::"n"(n) : "memory")
#define WAIT_L0() __builtin_amdgcn_s_waitcnt(0xC07F)
#define BAR() do { asm volatile("" ::: "memory"); __builtin_amdgcn_s_barrier(); asm volatile("" ::: "memory"); } while (0)

#define STAMP(i)
#define CSTAMP(i)
#define DIAG_ARG

__device__ __forceinline__ void wait_until(unsigned long long t0, unsigned ticks) {
  while (__builtin_amdgcn_s_memrealtime() - t0 < ticks) __builtin_amdgcn_s_sleep(1);
}

__global__ __launch_bounds__(512, 2) void k_fused(const u16* __restrict__ x, const int* __restrict__ qw, const u16* __restrict__ cb,
                                                  const u16* __restrict__ am, char* __restrict__ ws, u16* __restrict__ out DIAG_ARG) {
  extern __shared__ __attribute__((aligned(16))) char smem_g[];
  LAS char* smem = (LAS char*)smem_g;
  const unsigned long long T0 = __builtin_amdgcn_s_memrealtime();
  const int tid = threadIdx.x, lane = tid & 63;
  const int w = __builtin_amdgcn_readfirstlane(tid >> 6);
  const int b = blockIdx.x, xcd = b & 7, bi = b >> 3;
#define MAP 0
  const int mt = MAP ? (bi & 15) : (xcd & 1) * 8 + (bi & 7), nt = MAP ? xcd * 2 + (bi >> 4) : (xcd >> 1) * 4 + (bi >> 3);
  const int m0 = mt * BM, n0 = nt * BN;
  STAMP(0);
  const __amdgpu_buffer_rsrc_t rw = __builtin_amdgcn_make_buffer_rsrc((void*)ws, 0, EPOCH_OFF + 16 * 128, 0x00020000);
  const char* pe = ws + EPOCH_OFF + nt * 128;
  char* pfl = ws + FLAG_OFF + nt * 8 * 128 + mt * 8;

  const int cbk = lane < 56 ? lane : 55;
  const int* qsrc = qw + (size_t)(mt * 8) * N_ + n0 + cbk * 4;
  i32x4 q[8];
#define RAW_LOAD(g) do { const int* s_ = qsrc + (size_t)(g) * 128 * N_; _Pragma("unroll") for (int r = 0; r < 8; ++r) { const int* p_ = s_ + (size_t)r * N_; \
      asm volatile("global_load_dwordx4 %0, %1, off nt" : "=v"(q[r]) : "v"(p_) : "memory"); } } while (0)
#define SCALE_LOAD(dst, g) do { const u16* p_ = am + (size_t)(g) * N_ + n0 + cbk * 4; asm volatile("global_load_dwordx2 %0, %1, off" : "=v"(dst) : "v"(p_) : "memory"); } while (0)
#define E_LOAD() asm volatile("global_load_dwordx2 %0, %1, off sc1" : "=v"(E) : "v"(pe) : "memory")
#define FLAG_STORE(g) do { char* p_ = pfl + (g) * 128; asm volatile("global_store_dwordx2 %0, %1, off sc1" ::"v"(p_), "v"(tag) : "memory"); } while (0)
  auto dequant = [&](LAS char* scr, const u32x2 sc, int c0, int c1) {
#pragma unroll
    for (int r = 0; r < 8; ++r) asm volatile("" : "+v"(q[r]));
    const LAS float* lut = (const LAS float*)(scr + 3584);
    const float s[4] = {bf_lo(sc.x), bf_hi(sc.x), bf_lo(sc.y), bf_hi(sc.y)};
#pragma unroll
    for (int c = c0; c < c1; ++c) {
      u32x4 o;
#pragma unroll
      for (int pp = 0; pp < 4; ++pp) o[pp] = pk_bf16(lut[q[2 * pp][c]] * s[c], lut[q[2 * pp + 1][c]] * s[c]);
      if (lane < 56) *(LAS u32x4*)(scr + lane * 64 + c * 16) = o;
    }
  };
  auto publish = [&](LAS char* scr, int g) {
    const int so = nt * J_B + (g * 16 + mt) * KB_B;
#pragma unroll
    for (int s2 = 0; s2 < 4; ++s2) {
      const u32x4 v = *(const LAS u32x4*)(scr + s2 * 1024 + lane * 16);
      const int vo = ((s2 == 3 && lane >= 32) ? 0x7f000000 : lane * 16) + so + s2 * 1024;
      __builtin_amdgcn_raw_buffer_store_b128(v, rw, vo, 0, 16);
    }
  };
#define SELF_PF(g) if (SELFPF) __builtin_amdgcn_raw_ptr_buffer_load_lds(rw, (LAS void*)(smem + DUMMY_OFF), 4, lane < 28 ? lane * 128 : 0x7ffffff0, nt * J_B + ((g) * 16 + mt) * KB_B, 0, 16)
  u32x2 E, tag;
  unsigned cbv;

  if (w >= 6) {
    const int pw = w - 6;
    __builtin_amdgcn_s_setprio(3);
    LAS char* scr = smem + SCR_OFF + (4 + pw) * SCR_W;
    u32x2 sA, sB;
    E_LOAD();
    { const u16* pc = cb + (lane & 15); asm volatile("global_load_ushort %0, %1, off" : "=v"(cbv) : "v"(pc) : "memory"); }
    SCALE_LOAD(sA, 4 + pw);
    SCALE_LOAD(sB, 6 + pw);
    wait_until(T0, pw ? T_G5 : T_G4);
    RAW_LOAD(4 + pw);
    WAIT_V(8);
    asm volatile("" : "+v"(E), "+v"(cbv), "+v"(sA), "+v"(sB));
    tag.x = E.x + 1u;
    tag.y = E.y ^ 0x5bd1e995u;
    if (lane < 16) *(LAS float*)(scr + 3584 + lane * 4) = __uint_as_float(cbv << 16);
    WAIT_V(0);
    STAMP(1);
    dequant(scr, sA, 0, 4);
    publish(scr, 4 + pw);
    wait_until(T0, pw ? T_G7 : T_G6);
    RAW_LOAD(6 + pw);
    WAIT_V(8);
    FLAG_STORE(4 + pw);
    STAMP(2);
    WAIT_V(1);
    STAMP(4);
    dequant(scr, sB, 0, 4);
    publish(scr, 6 + pw);
    STAMP(3);
#define PBAR(t) do { STAMP(9 + (t)); BAR(); } while (0)
    BAR();
    PBAR(0);
    WAIT_V(0);
    FLAG_STORE(6 + pw);
    PBAR(1); PBAR(2); PBAR(3); PBAR(4); PBAR(5); PBAR(6); PBAR(7);
    PBAR(8); PBAR(9); PBAR(10); PBAR(11); PBAR(12); PBAR(13); PBAR(14); PBAR(15);
  } else if (w >= 4) {
    const int lw = w - 4;
    const __amdgpu_buffer_rsrc_t rx = __builtin_amdgcn_make_buffer_rsrc((void*)x, 0, M_ * K_ * 2, 0x00020000);
    const int p = lane & 7, rl = lane >> 3, h = lane >> 4;
    int vx[2];
#pragma unroll
    for (int par = 0; par < 2; ++par) vx[par] = rl * (K_ * 2) + (p ^ (4 * par + h)) * 16;
    const int vw = lane * 16;
    const int sx0 = (m0 + lw * 64) * (K_ * 2);
    const int sw0 = nt * J_B + lw * 14 * 1024;
    auto issue_xwarm = [&](int piece) {
      __builtin_amdgcn_raw_ptr_buffer_load_lds(rx, (LAS void*)(smem + DUMMY_OFF), 4, lane < 32 ? (lane >> 2) * (K_ * 2) + (lane & 3) * 128 : 0x7ffffff0,
                                               b * 8 * (K_ * 2) + piece * 512, 0, 0);
    };
    auto issue_x = [&](int kt, int slot) {
      LAS char* base = smem + slot * SLOT_B;
#pragma unroll
      for (int i = 0; i < 8; ++i)
        __builtin_amdgcn_raw_ptr_buffer_load_lds(rx, (LAS void*)(base + (lw * 8 + i) * 1024), 16, vx[i & 1], sx0 + i * 8 * (K_ * 2) + kt * 128, 0, 0);
    };
    auto issue_w = [&](int kt, int slot) {
      LAS char* base = smem + slot * SLOT_B;
#pragma unroll
      for (int i = 0; i < 14; ++i)
        __builtin_amdgcn_raw_ptr_buffer_load_lds(rw, (LAS void*)(base + XT_B + (lw * 14 + i) * 1024), 16, vw, sw0 + i * 1024 + kt * (8 * KB_B), 0, 16);
    };
    const char* pf = ws + FLAG_OFF + nt * 8 * 128 + (lane & 15) * 8;
    u32x2 Fa, Fb, Fs;
#define POLL_ISSUE(F, g) do { const char* pp_ = pf + (g) * 128; asm volatile("global_load_dwordx2 %0, %1, off sc1" : "=v"(F) : "v"(pp_) : "memory"); } while (0)
#define POLL_OK(F) (__builtin_amdgcn_ballot_w64(F.x == tag.x && F.y == tag.y) == ~0ull)
#define PIN(F) asm volatile("" : "+v"(F))
    E_LOAD();
    wait_until(T0, lw ? T_XWB : T_XWA);
    issue_xwarm(lw);
    WAIT_V(1);
    asm volatile("" : "+v"(E));
    tag.x = E.x + 1u;
    tag.y = E.y ^ 0x5bd1e995u;
    wait_until(T0, T_POLL);
    STAMP(1);
    auto spin = [&](int g) {
      const unsigned long long t0 = __builtin_amdgcn_s_memrealtime();
      for (;;) {
        POLL_ISSUE(Fs, g);
        WAIT_V(0);
        PIN(Fs);
        if (POLL_OK(Fs)) break;
        if (__builtin_amdgcn_s_memrealtime() - t0 > 2000000ull) break;
        __builtin_amdgcn_s_sleep(1);
      }
    };
    spin(0);
    STAMP(2);
    issue_x(0, 0);
    issue_w(0, 0);
    issue_x(1, 1);
    issue_w(1, 1);
    POLL_ISSUE(Fa, 1);
    POLL_ISSUE(Fb, 2);
    issue_xwarm(2 + lw);
    WAIT_V(25);
    STAMP(3);
    BAR();
    WAIT_V(1);
    PIN(Fa); PIN(Fb);
    if (!POLL_OK(Fa)) spin(1);
    int slot = 2;
#define TRIP(j, FNEW, FOLD) do { \
      issue_x(2 * (j) + 2, slot); \
      issue_w(2 * (j) + 2, slot); \
      slot = slot == 2 ? 0 : slot + 1; \
      WAIT_V(23); \
      STAMP(9 + 2 * (j)); \
      BAR(); \
      issue_x(2 * (j) + 3, slot); \
      issue_w(2 * (j) + 3, slot); \
      slot = slot == 2 ? 0 : slot + 1; \
      POLL_ISSUE(FNEW, (j) + 3 < 8 ? (j) + 3 : 7); \
      WAIT_V(23); \
      PIN(FOLD); \
      const bool ok_ = POLL_OK(FOLD); \
      STAMP(10 + 2 * (j)); \
      BAR(); \
      if (!ok_ && (j) + 2 < 8) spin((j) + 2); \
    } while (0)
    TRIP(0, Fa, Fb);
#pragma unroll 1
    for (int j = 1; j < 7; j += 2) {
      TRIP(j, Fb, Fa);
      if (j == 5 && mt == 0 && lw == 0 && lane == 0) {
        const u32x2 ne = {E.x + 1u, E.y};
        asm volatile("global_store_dwordx2 %0, %1, off sc1" ::"v"(pe), "v"(ne) : "memory");
      }
      TRIP(j + 1, Fa, Fb);
    }
    WAIT_V(0);
    BAR();
    BAR();
  } else {
    const int wm = w & 1, wn = w >> 1;
    {
      LAS char* scr = smem + SCR_OFF + w * SCR_W;
      u32x2 sA;
      E_LOAD();
      { const u16* pc = cb + (lane & 15); asm volatile("global_load_ushort %0, %1, off" : "=v"(cbv) : "v"(pc) : "memory"); }
      SCALE_LOAD(sA, w);
      if (w) wait_until(T0, w == 1 ? T_G1 : w == 2 ? T_G2 : T_G3);
      RAW_LOAD(w);
      WAIT_V(8);
      asm volatile("" : "+v"(E), "+v"(cbv), "+v"(sA));
      tag.x = E.x + 1u;
      tag.y = E.y ^ 0x5bd1e995u;
      if (lane < 16) *(LAS float*)(scr + 3584 + lane * 4) = __uint_as_float(cbv << 16);
      WAIT_V(0);
      STAMP(1);
      dequant(scr, sA, 0, 4);
      publish(scr, w);
      STAMP(2);
      WAIT_V(0);
      FLAG_STORE(w);
      SELF_PF(w);
      STAMP(3);
    }
    const int fr = lane & 15, fh = lane >> 4;
    const int xa0 = (wm * 64 + fr) * 128 + (fh ^ (fr >> 1)) * 16;
    const int wa0 = XT_B + fh * KB_B + (wn * 112 + fr) * 16;
    f32x4 acc[4][7];
#pragma unroll
    for (int mi = 0; mi < 4; ++mi)
#pragma unroll
      for (int ni = 0; ni < 7; ++ni) acc[mi][ni] = (f32x4){0.f, 0.f, 0.f, 0.f};
    bf16x8 X0[4], W0[7], X1[4], W1[7];
#define RD(XF, WF, so, ks) do { \
    _Pragma("unroll") for (int ni = 0; ni < 7; ++ni) WF[ni] = *(const LAS bf16x8*)(smem + (so) + wa0 + (ks) * 4 * KB_B + ni * 256); \
    _Pragma("unroll") for (int mi = 0; mi < 4; ++mi) XF[mi] = *(const LAS bf16x8*)(smem + (so) + ((xa0 ^ ((ks) * 64))) + mi * 2048); } while (0)
#define MM(XF, WF) do { \
    _Pragma("unroll") for (int mi = 0; mi < 4; ++mi) _Pragma("unroll") for (int ni = 0; ni < 7; ++ni) \
      acc[mi][ni] = __builtin_amdgcn_mfma_f32_16x16x32_bf16(WF[ni], XF[mi], acc[mi][ni], 0, 0, 0); } while (0)
#define ILV() do { _Pragma("unroll") for (int g_ = 0; g_ < 11; ++g_) { \
      __builtin_amdgcn_sched_group_barrier(0x100, 1, 0); __builtin_amdgcn_sched_group_barrier(0x8, 2, 0); } \
      __builtin_amdgcn_sched_group_barrier(0x8, 6, 0); } while (0)
    STAMP(4);
    BAR();
    STAMP(8);
    CSTAMP(26);
    int so = 0;
    RD(X0, W0, so, 0);
#pragma unroll 1
    for (int t = 0; t < 15; ++t) {
      WAIT_L0();
      __builtin_amdgcn_sched_barrier(0);
      RD(X1, W1, so, 1);
      MM(X0, W0);
      ILV();
      __builtin_amdgcn_sched_barrier(0);
      WAIT_L0();
      STAMP(9 + t);
      BAR();
      __builtin_amdgcn_sched_barrier(0);
      so = so == 2 * SLOT_B ? 0 : so + SLOT_B;
      RD(X0, W0, so, 0);
      MM(X1, W1);
      ILV();
      __builtin_amdgcn_sched_barrier(0);
    }
    WAIT_L0();
    __builtin_amdgcn_sched_barrier(0);
    RD(X1, W1, so, 1);
    MM(X0, W0);
    ILV();
    __builtin_amdgcn_sched_barrier(0);
    WAIT_L0();
    STAMP(24);
    BAR();
    MM(X1, W1);
    STAMP(5);
    CSTAMP(27);
#pragma unroll
    for (int mi = 0; mi < 4; ++mi)
#pragma unroll
      for (int ni = 0; ni < 7; ++ni) {
        u32x2 o = {pk_bf16(acc[mi][ni][0], acc[mi][ni][1]), pk_bf16(acc[mi][ni][2], acc[mi][ni][3])};
        *(LAS u32x2*)(smem + (wm * 64 + mi * 16 + fr) * OPITCH + (wn * 112 + ni * 16 + fh * 4) * 2) = o;
      }
  }
  WAIT_L0();
  BAR();
#pragma unroll
  for (int r = 0; r < 7; ++r) {
    const int c = r * 512 + tid;
    const int row = c / 28, ch = c - row * 28;
    const u32x4 v = *(const LAS u32x4*)(smem + row * OPITCH + ch * 16);
    __builtin_nontemporal_store(v, (u32x4*)((char*)out + (size_t)(m0 + row) * (N_ * 2) + n0 * 2 + ch * 16));
  }
  STAMP(6);
}

extern "C" void kernel_launch(void* const* d_in, const int* in_sizes, int n_in, void* d_out, int out_size, void* d_ws, size_t ws_size,
                              hipStream_t stream) {
  const u16* x = (const u16*)d_in[0];
  const int* qw = (const int*)d_in[1];
  const u16* cb = (const u16*)d_in[2];
  const u16* am = (const u16*)d_in[3];
  (void)hipFuncSetAttribute((const void*)k_fused, hipFuncAttributeMaxDynamicSharedMemorySize, LDS_B);
  k_fused<<<256, 512, LDS_B, stream>>>(x, qw, cb, am, (char*)d_ws, (u16*)d_out);
}
```

```cpp
#define SELFT
#define SELFB 77
#define SELFW 0
#define TX 1
#define SCALE 30
#include <hip/hip_runtime.h>
#include <stdint.h>
typedef unsigned short u16;
typedef __attribute__((ext_vector_type(8))) __bf16 bf16x8;
typedef __attribute__((ext_vector_type(2))) __bf16 bf16x2;
typedef float f32x4 __attribute__((ext_vector_type(4)));
typedef float f32x2 __attribute__((ext_vector_type(2)));
typedef int i32x4 __attribute__((ext_vector_type(4)));
typedef unsigned u32x4 __attribute__((ext_vector_type(4)));
typedef unsigned u32x2 __attribute__((ext_vector_type(2)));
#define LAS __attribute__((address_space(3)))

constexpr int M_ = 2048, N_ = 3584, K_ = 1024;
constexpr int BM = 128, BN = 224;
constexpr int XT_B = BM * 64 * 2;
constexpr int WT_B = BN * 64 * 2;
constexpr int SLOT_B = XT_B + WT_B;
constexpr int NSLOT = 3;
constexpr int KB_B = BN * 16;
constexpr int J_B = (K_ / 8) * KB_B;
constexpr int W_TOTAL = (N_ / BN) * J_B;
constexpr int FLAG_OFF = W_TOTAL;
constexpr int EPOCH_OFF = FLAG_OFF + 128 * 128;
constexpr int SCR_OFF = NSLOT * SLOT_B;
constexpr int SCR_W = 3712;
constexpr int DUMMY_OFF = SCR_OFF + 6 * SCR_W;
constexpr int SIG_OFF = DUMMY_OFF + 256;
constexpr int LDS_B = SIG_OFF + 16;
constexpr int OPITCH = 464;

#define T_G1 45
#define T_G2 70
#define T_G3 95
#define T_G4 60
#define T_G5 85
#define T_G6 0
#define T_G7 0
#define T_XWA 25
#define T_XWB 60
#define T_POLL 200
#define SELFPF 0

__device__ __forceinline__ unsigned pk_bf16(float a, float b) {
  f32x2 v = {a, b};
  bf16x2 r = __builtin_convertvector(v, bf16x2);
  return __builtin_bit_cast(unsigned, r);
}
__device__ __forceinline__ float bf_lo(unsigned u) { return __uint_as_float(u << 16); }
__device__ __forceinline__ float bf_hi(unsigned u) { return __uint_as_float(u & 0xffff0000u); }

#define WAIT_V(n) asm volatile("s_waitcnt vmcnt(%0)" ::"n"(n) : "memory")
#define WAIT_L0() __builtin_amdgcn_s_waitcnt(0xC07F)
#define BAR() do { asm volatile("" ::: "memory"); __builtin_amdgcn_s_barrier(); asm volatile("" ::: "memory"); } while (0)

#define STAMP(i) do { if ((i) == TX) TXV = __builtin_amdgcn_s_memrealtime(); } while (0)
#define CSTAMP(i)
#define DIAG_ARG

__device__ __forceinline__ void wait_until(unsigned long long t0, unsigned ticks) {
  while (__builtin_amdgcn_s_memrealtime() - t0 < ticks) __builtin_amdgcn_s_sleep(1);
}

__global__ __launch_bounds__(512, 2) void k_fused(const u16* __restrict__ x, const int* __restrict__ qw, const u16* __restrict__ cb,
                                                  const u16* __restrict__ am, char* __restrict__ ws, u16* __restrict__ out DIAG_ARG) {
  extern __shared__ __attribute__((aligned(16))) char smem_g[];
  LAS char* smem = (LAS char*)smem_g;
  const unsigned long long T0 = __builtin_amdgcn_s_memrealtime();
  const int tid = threadIdx.x, lane = tid & 63;
  const int w = __builtin_amdgcn_readfirstlane(tid >> 6);
  const int b = blockIdx.x, xcd = b & 7, bi = b >> 3;
#define MAP 0
  const int mt = MAP ? (bi & 15) : (xcd & 1) * 8 + (bi & 7), nt = MAP ? xcd * 2 + (bi >> 4) : (xcd >> 1) * 4 + (bi >> 3);
  const int m0 = mt * BM, n0 = nt * BN;
  unsigned long long TXV = T0;
  STAMP(0);
  const __amdgpu_buffer_rsrc_t rw = __builtin_amdgcn_make_buffer_rsrc((void*)ws, 0, EPOCH_OFF + 16 * 128, 0x00020000);
  const char* pe = ws + EPOCH_OFF + nt * 128;
  char* pfl = ws + FLAG_OFF + nt * 8 * 128 + mt * 8;

  const int cbk = lane < 56 ? lane : 55;
  const int* qsrc = qw + (size_t)(mt * 8) * N_ + n0 + cbk * 4;
  i32x4 q[8];
#define RAW_LOAD(g) do { const int* s_ = qsrc + (size_t)(g) * 128 * N_; _Pragma("unroll") for (int r = 0; r < 8; ++r) { const int* p_ = s_ + (size_t)r * N_; \
      asm volatile("global_load_dwordx4 %0, %1, off nt" : "=v"(q[r]) : "v"(p_) : "memory"); } } while (0)
#define SCALE_LOAD(dst, g) do { const u16* p_ = am + (size_t)(g) * N_ + n0 + cbk * 4; asm volatile("global_load_dwordx2 %0, %1, off" : "=v"(dst) : "v"(p_) : "memory"); } while (0)
#define E_LOAD() asm volatile("global_load_dwordx2 %0, %1, off sc1" : "=v"(E) : "v"(pe) : "memory")
#define FLAG_STORE(g) do { char* p_ = pfl + (g) * 128; asm volatile("global_store_dwordx2 %0, %1, off sc1" ::"v"(p_), "v"(tag) : "memory"); } while (0)
  auto dequant = [&](LAS char* scr, const u32x2 sc, int c0, int c1) {
#pragma unroll
    for (int r = 0; r < 8; ++r) asm volatile("" : "+v"(q[r]));
    const LAS float* lut = (const LAS float*)(scr + 3584);
    const float s[4] = {bf_lo(sc.x), bf_hi(sc.x), bf_lo(sc.y), bf_hi(sc.y)};
#pragma unroll
    for (int c = c0; c < c1; ++c) {
      u32x4 o;
#pragma unroll
      for (int pp = 0; pp < 4; ++pp) o[pp] = pk_bf16(lut[q[2 * pp][c]] * s[c], lut[q[2 * pp + 1][c]] * s[c]);
      if (lane < 56) *(LAS u32x4*)(scr + lane * 64 + c * 16) = o;
    }
  };
  auto publish = [&](LAS char* scr, int g) {
    const int so = nt * J_B + (g * 16 + mt) * KB_B;
#pragma unroll
    for (int s2 = 0; s2 < 4; ++s2) {
      const u32x4 v = *(const LAS u32x4*)(scr + s2 * 1024 + lane * 16);
      const int vo = ((s2 == 3 && lane >= 32) ? 0x7f000000 : lane * 16) + so + s2 * 1024;
      __builtin_amdgcn_raw_buffer_store_b128(v, rw, vo, 0, 16);
    }
  };
#define SELF_PF(g) if (SELFPF) __builtin_amdgcn_raw_ptr_buffer_load_lds(rw, (LAS void*)(smem + DUMMY_OFF), 4, lane < 28 ? lane * 128 : 0x7ffffff0, nt * J_B + ((g) * 16 + mt) * KB_B, 0, 16)
  u32x2 E, tag;
  unsigned cbv;

  if (w >= 6) {
    const int pw = w - 6;
    __builtin_amdgcn_s_setprio(3);
    LAS char* scr = smem + SCR_OFF + (4 + pw) * SCR_W;
    u32x2 sA, sB;
    E_LOAD();
    { const u16* pc = cb + (lane & 15); asm volatile("global_load_ushort %0, %1, off" : "=v"(cbv) : "v"(pc) : "memory"); }
    SCALE_LOAD(sA, 4 + pw);
    SCALE_LOAD(sB, 6 + pw);
    wait_until(T0, pw ? T_G5 : T_G4);
    RAW_LOAD(4 + pw);
    WAIT_V(8);
    asm volatile("" : "+v"(E), "+v"(cbv), "+v"(sA), "+v"(sB));
    tag.x = E.x + 1u;
    tag.y = E.y ^ 0x5bd1e995u;
    if (lane < 16) *(LAS float*)(scr + 3584 + lane * 4) = __uint_as_float(cbv << 16);
    WAIT_V(0);
    STAMP(1);
    dequant(scr, sA, 0, 4);
    publish(scr, 4 + pw);
    wait_until(T0, pw ? T_G7 : T_G6);
    RAW_LOAD(6 + pw);
    WAIT_V(8);
    FLAG_STORE(4 + pw);
    STAMP(2);
    WAIT_V(1);
    STAMP(4);
    dequant(scr, sB, 0, 4);
    publish(scr, 6 + pw);
    STAMP(3);
#define PBAR(t) do { STAMP(9 + (t)); BAR(); } while (0)
    BAR();
    PBAR(0);
    WAIT_V(0);
    FLAG_STORE(6 + pw);
    PBAR(1); PBAR(2); PBAR(3); PBAR(4); PBAR(5); PBAR(6); PBAR(7);
    PBAR(8); PBAR(9); PBAR(10); PBAR(11); PBAR(12); PBAR(13); PBAR(14); PBAR(15);
  } else if (w >= 4) {
    const int lw = w - 4;
    const __amdgpu_buffer_rsrc_t rx = __builtin_amdgcn_make_buffer_rsrc((void*)x, 0, M_ * K_ * 2, 0x00020000);
    const int p = lane & 7, rl = lane >> 3, h = lane >> 4;
    int vx[2];
#pragma unroll
    for (int par = 0; par < 2; ++par) vx[par] = rl * (K_ * 2) + (p ^ (4 * par + h)) * 16;
    const int vw = lane * 16;
    const int sx0 = (m0 + lw * 64) * (K_ * 2);
    const int sw0 = nt * J_B + lw * 14 * 1024;
    auto issue_xwarm = [&](int piece) {
      __builtin_amdgcn_raw_ptr_buffer_load_lds(rx, (LAS void*)(smem + DUMMY_OFF), 4, lane < 32 ? (lane >> 2) * (K_ * 2) + (lane & 3) * 128 : 0x7ffffff0,
                                               b * 8 * (K_ * 2) + piece * 512, 0, 0);
    };
    auto issue_x = [&](int kt, int slot) {
      LAS char* base = smem + slot * SLOT_B;
#pragma unroll
      for (int i = 0; i < 8; ++i)
        __builtin_amdgcn_raw_ptr_buffer_load_lds(rx, (LAS void*)(base + (lw * 8 + i) * 1024), 16, vx[i & 1], sx0 + i * 8 * (K_ * 2) + kt * 128, 0, 0);
    };
    auto issue_w = [&](int kt, int slot) {
      LAS char* base = smem + slot * SLOT_B;
#pragma unroll
      for (int i = 0; i < 14; ++i)
        __builtin_amdgcn_raw_ptr_buffer_load_lds(rw, (LAS void*)(base + XT_B + (lw * 14 + i) * 1024), 16, vw, sw0 + i * 1024 + kt * (8 * KB_B), 0, 16);
    };
    const char* pf = ws + FLAG_OFF + nt * 8 * 128 + (lane & 15) * 8;
    u32x2 Fa, Fb, Fs;
#define POLL_ISSUE(F, g) do { const char* pp_ = pf + (g) * 128; asm volatile("global_load_dwordx2 %0, %1, off sc1" : "=v"(F) : "v"(pp_) : "memory"); } while (0)
#define POLL_OK(F) (__builtin_amdgcn_ballot_w64(F.x == tag.x && F.y == tag.y) == ~0ull)
#define PIN(F) asm volatile("" : "+v"(F))
    E_LOAD();
    wait_until(T0, lw ? T_XWB : T_XWA);
    issue_xwarm(lw);
    WAIT_V(1);
    asm volatile("" : "+v"(E));
    tag.x = E.x + 1u;
    tag.y = E.y ^ 0x5bd1e995u;
    wait_until(T0, T_POLL);
    STAMP(1);
    auto spin = [&](int g) {
      const unsigned long long t0 = __builtin_amdgcn_s_memrealtime();
      for (;;) {
        POLL_ISSUE(Fs, g);
        WAIT_V(0);
        PIN(Fs);
        if (POLL_OK(Fs)) break;
        if (__builtin_amdgcn_s_memrealtime() - t0 > 2000000ull) break;
        __builtin_amdgcn_s_sleep(1);
      }
    };
    spin(0);
    STAMP(2);
    issue_x(0, 0);
    issue_w(0, 0);
    issue_x(1, 1);
    issue_w(1, 1);
    POLL_ISSUE(Fa, 1);
    POLL_ISSUE(Fb, 2);
    issue_xwarm(2 + lw);
    WAIT_V(25);
    STAMP(3);
    BAR();
    WAIT_V(1);
    PIN(Fa); PIN(Fb);
    if (!POLL_OK(Fa)) spin(1);
    int slot = 2;
#define TRIP(j, FNEW, FOLD) do { \
      issue_x(2 * (j) + 2, slot); \
      issue_w(2 * (j) + 2, slot); \
      slot = slot == 2 ? 0 : slot + 1; \
      WAIT_V(23); \
      STAMP(9 + 2 * (j)); \
      BAR(); \
      issue_x(2 * (j) + 3, slot); \
      issue_w(2 * (j) + 3, slot); \
      slot = slot == 2 ? 0 : slot + 1; \
      POLL_ISSUE(FNEW, (j) + 3 < 8 ? (j) + 3 : 7); \
      WAIT_V(23); \
      PIN(FOLD); \
      const bool ok_ = POLL_OK(FOLD); \
      STAMP(10 + 2 * (j)); \
      BAR(); \
      if (!ok_ && (j) + 2 < 8) spin((j) + 2); \
    } while (0)
    TRIP(0, Fa, Fb);
#pragma unroll 1
    for (int j = 1; j < 7; j += 2) {
      TRIP(j, Fb, Fa);
      if (j == 5 && mt == 0 && lw == 0 && lane == 0) {
        const u32x2 ne = {E.x + 1u, E.y};
        asm volatile("global_store_dwordx2 %0, %1, off sc1" ::"v"(pe), "v"(ne) : "memory");
      }
      TRIP(j + 1, Fa, Fb);
    }
    WAIT_V(0);
    BAR();
    BAR();
  } else {
    const int wm = w & 1, wn = w >> 1;
    {
      LAS char* scr = smem + SCR_OFF + w * SCR_W;
      u32x2 sA;
      E_LOAD();
      { const u16* pc = cb + (lane & 15); asm volatile("global_load_ushort %0, %1, off" : "=v"(cbv) : "v"(pc) : "memory"); }
      SCALE_LOAD(sA, w);
      if (w) wait_until(T0, w == 1 ? T_G1 : w == 2 ? T_G2 : T_G3);
      RAW_LOAD(w);
      WAIT_V(8);
      asm volatile("" : "+v"(E), "+v"(cbv), "+v"(sA));
      tag.x = E.x + 1u;
      tag.y = E.y ^ 0x5bd1e995u;
      if (lane < 16) *(LAS float*)(scr + 3584 + lane * 4) = __uint_as_float(cbv << 16);
      WAIT_V(0);
      STAMP(1);
      dequant(scr, sA, 0, 4);
      publish(scr, w);
      STAMP(2);
      WAIT_V(0);
      FLAG_STORE(w);
      SELF_PF(w);
      STAMP(3);
    }
    const int fr = lane & 15, fh = lane >> 4;
    const int xa0 = (wm * 64 + fr) * 128 + (fh ^ (fr >> 1)) * 16;
    const int wa0 = XT_B + fh * KB_B + (wn * 112 + fr) * 16;
    f32x4 acc[4][7];
#pragma unroll
    for (int mi = 0; mi < 4; ++mi)
#pragma unroll
      for (int ni = 0; ni < 7; ++ni) acc[mi][ni] = (f32x4){0.f, 0.f, 0.f, 0.f};
    bf16x8 X0[4], W0[7], X1[4], W1[7];
#define RD(XF, WF, so, ks) do { \
    _Pragma("unroll") for (int ni = 0; ni < 7; ++ni) WF[ni] = *(const LAS bf16x8*)(smem + (so) + wa0 + (ks) * 4 * KB_B + ni * 256); \
    _Pragma("unroll") for (int mi = 0; mi < 4; ++mi) XF[mi] = *(const LAS bf16x8*)(smem + (so) + ((xa0 ^ ((ks) * 64))) + mi * 2048); } while (0)
#define MM(XF, WF) do { \
    _Pragma("unroll") for (int mi = 0; mi < 4; ++mi) _Pragma("unroll") for (int ni = 0; ni < 7; ++ni) \
      acc[mi][ni] = __builtin_amdgcn_mfma_f32_16x16x32_bf16(WF[ni], XF[mi], acc[mi][ni], 0, 0, 0); } while (0)
#define ILV() do { _Pragma("unroll") for (int g_ = 0; g_ < 11; ++g_) { \
      __builtin_amdgcn_sched_group_barrier(0x100, 1, 0); __builtin_amdgcn_sched_group_barrier(0x8, 2, 0); } \
      __builtin_amdgcn_sched_group_barrier(0x8, 6, 0); } while (0)
    STAMP(4);
    BAR();
    STAMP(8);
    CSTAMP(26);
    int so = 0;
    RD(X0, W0, so, 0);
#pragma unroll 1
    for (int t = 0; t < 15; ++t) {
      WAIT_L0();
      __builtin_amdgcn_sched_barrier(0);
      RD(X1, W1, so, 1);
      MM(X0, W0);
      ILV();
      __builtin_amdgcn_sched_barrier(0);
      WAIT_L0();
      STAMP(9 + t);
      BAR();
      __builtin_amdgcn_sched_barrier(0);
      so = so == 2 * SLOT_B ? 0 : so + SLOT_B;
      RD(X0, W0, so, 0);
      MM(X1, W1);
      ILV();
      __builtin_amdgcn_sched_barrier(0);
    }
    WAIT_L0();
    __builtin_amdgcn_sched_barrier(0);
    RD(X1, W1, so, 1);
    MM(X0, W0);
    ILV();
    __builtin_amdgcn_sched_barrier(0);
    WAIT_L0();
    STAMP(24);
    BAR();
    MM(X1, W1);
    STAMP(5);
    CSTAMP(27);
#pragma unroll
    for (int mi = 0; mi < 4; ++mi)
#pragma unroll
      for (int ni = 0; ni < 7; ++ni) {
        u32x2 o = {pk_bf16(acc[mi][ni][0], acc[mi][ni][1]), pk_bf16(acc[mi][ni][2], acc[mi][ni][3])};
        *(LAS u32x2*)(smem + (wm * 64 + mi * 16 + fr) * OPITCH + (wn * 112 + ni * 16 + fh * 4) * 2) = o;
      }
  }
  WAIT_L0();
  BAR();
#pragma unroll
  for (int r = 0; r < 7; ++r) {
    const int c = r * 512 + tid;
    const int row = c / 28, ch = c - row * 28;
    const u32x4 v = *(const LAS u32x4*)(smem + row * OPITCH + ch * 16);
    __builtin_nontemporal_store(v, (u32x4*)((char*)out + (size_t)(m0 + row) * (N_ * 2) + n0 * 2 + ch * 16));
  }
  STAMP(6);
  if (b == SELFB && w == SELFW) {
    const unsigned long long d = (TXV - T0) * SCALE, t1 = __builtin_amdgcn_s_memrealtime();
    while (__builtin_amdgcn_s_memrealtime() - t1 < d) __builtin_amdgcn_s_sleep(1);
  }
}

extern "C" void kernel_launch(void* const* d_in, const int* in_sizes, int n_in, void* d_out, int out_size, void* d_ws, size_t ws_size,
                              hipStream_t stream) {
  const u16* x = (const u16*)d_in[0];
  const int* qw = (const int*)d_in[1];
  const u16* cb = (const u16*)d_in[2];
  const u16* am = (const u16*)d_in[3];
  (void)hipFuncSetAttribute((const void*)k_fused, hipFuncAttributeMaxDynamicSharedMemorySize, LDS_B);
  k_fused<<<256, 512, LDS_B, stream>>>(x, qw, cb, am, (char*)d_ws, (u16*)d_out);
}
```

```cpp
#define T_POLL 100
#define T_G1 30
#define T_G2 50
#define T_G3 70
#define T_G4 40
#define T_G5 60
#define T_G6 80
#define T_G7 100
#include <hip/hip_runtime.h>
#include <stdint.h>
typedef unsigned short u16;
typedef __attribute__((ext_vector_type(8))) __bf16 bf16x8;
typedef __attribute__((ext_vector_type(2))) __bf16 bf16x2;
typedef float f32x4 __attribute__((ext_vector_type(4)));
typedef float f32x2 __attribute__((ext_vector_type(2)));
typedef int i32x4 __attribute__((ext_vector_type(4)));
typedef unsigned u32x4 __attribute__((ext_vector_type(4)));
typedef unsigned u32x2 __attribute__((ext_vector_type(2)));
#define LAS __attribute__((address_space(3)))

constexpr int M_ = 2048, N_ = 3584, K_ = 1024;
constexpr int BM = 128, BN = 224;
constexpr int XT_B = BM * 64 * 2;
constexpr int WT_B = BN * 64 * 2;
constexpr int SLOT_B = XT_B + WT_B;
constexpr int NSLOT = 3;
constexpr int KB_B = BN * 16;
constexpr int J_B = (K_ / 8) * KB_B;
constexpr int W_TOTAL = (N_ / BN) * J_B;
constexpr int FLAG_OFF = W_TOTAL;
constexpr int EPOCH_OFF = FLAG_OFF + 128 * 128;
constexpr int SCR_OFF = NSLOT * SLOT_B;
constexpr int SCR_W = 3712;
constexpr int DUMMY_OFF = SCR_OFF + 6 * SCR_W;
constexpr int SIG_OFF = DUMMY_OFF + 256;
constexpr int LDS_B = SIG_OFF + 16;
constexpr int OPITCH = 464;

#define T_XWA 25
#define T_XWB 60
#define SELFPF 0

__device__ __forceinline__ unsigned pk_bf16(float a, float b) {
  f32x2 v = {a, b};
  bf16x2 r = __builtin_convertvector(v, bf16x2);
  return __builtin_bit_cast(unsigned, r);
}
__device__ __forceinline__ float bf_lo(unsigned u) { return __uint_as_float(u << 16); }
__device__ __forceinline__ float bf_hi(unsigned u) { return __uint_as_float(u & 0xffff0000u); }

#define WAIT_V(n) asm volatile("s_waitcnt vmcnt(%0)" ::"n"(n) : "memory")
#define WAIT_L0() __builtin_amdgcn_s_waitcnt(0xC07F)
#define BAR() do { asm volatile("" ::: "memory"); __builtin_amdgcn_s_barrier(); asm volatile("" ::: "memory"); } while (0)

#define STAMP(i)
#define CSTAMP(i)
#define DIAG_ARG

__device__ __forceinline__ void wait_until(unsigned long long t0, unsigned ticks) {
  while (__builtin_amdgcn_s_memrealtime() - t0 < ticks) __builtin_amdgcn_s_sleep(1);
}

__global__ __launch_bounds__(512, 2) void k_fused(const u16* __restrict__ x, const int* __restrict__ qw, const u16* __restrict__ cb,
                                                  const u16* __restrict__ am, char* __restrict__ ws, u16* __restrict__ out DIAG_ARG) {
  extern __shared__ __attribute__((aligned(16))) char smem_g[];
  LAS char* smem = (LAS char*)smem_g;
  const unsigned long long T0 = __builtin_amdgcn_s_memrealtime();
  const int tid = threadIdx.x, lane = tid & 63;
  const int w = __builtin_amdgcn_readfirstlane(tid >> 6);
  const int b = blockIdx.x, xcd = b & 7, bi = b >> 3;
#define MAP 0
  const int mt = MAP ? (bi & 15) : (xcd & 1) * 8 + (bi & 7), nt = MAP ? xcd * 2 + (bi >> 4) : (xcd >> 1) * 4 + (bi >> 3);
  const int m0 = mt * BM, n0 = nt * BN;
  STAMP(0);
  const __amdgpu_buffer_rsrc_t rw = __builtin_amdgcn_make_buffer_rsrc((void*)ws, 0, EPOCH_OFF + 16 * 128, 0x00020000);
  const char* pe = ws + EPOCH_OFF + nt * 128;
  char* pfl = ws + FLAG_OFF + nt * 8 * 128 + mt * 8;

  const int cbk = lane < 56 ? lane : 55;
  const int* qsrc = qw + (size_t)(mt * 8) * N_ + n0 + cbk * 4;
#define RAW_LOAD(q, g) do { const int* s_ = qsrc + (size_t)(g) * 128 * N_; _Pragma("unroll") for (int r = 0; r < 8; ++r) { const int* p_ = s_ + (size_t)r * N_; \
      asm volatile("global_load_dwordx4 %0, %1, off nt" : "=v"(q[r]) : "v"(p_) : "memory"); } } while (0)
#define SCALE_LOAD(dst, g) do { const u16* p_ = am + (size_t)(g) * N_ + n0 + cbk * 4; asm volatile("global_load_dwordx2 %0, %1, off" : "=v"(dst) : "v"(p_) : "memory"); } while (0)
#define E_LOAD() asm volatile("global_load_dwordx2 %0, %1, off sc1" : "=v"(E) : "v"(pe) : "memory")
#define FLAG_STORE(g) do { char* p_ = pfl + (g) * 128; asm volatile("global_store_dwordx2 %0, %1, off sc1" ::"v"(p_), "v"(tag) : "memory"); } while (0)
  auto dequant2 = [&](i32x4 (&q)[8], LAS char* scr, const u32x2 sc, int c0) {
#pragma unroll
    for (int r = 0; r < 8; ++r) asm volatile("" : "+v"(q[r]));
    const LAS float* lut = (const LAS float*)(scr + 3584);
    float f[2][8];
#pragma unroll
    for (int c = 0; c < 2; ++c)
#pragma unroll
      for (int r = 0; r < 8; ++r) f[c][r] = lut[q[r][c0 + c]];
    __builtin_amdgcn_sched_group_barrier(0x2, 16, 0);
    __builtin_amdgcn_sched_group_barrier(0x100, 16, 0);
    const float s0 = c0 ? bf_lo(sc.y) : bf_lo(sc.x), s1 = c0 ? bf_hi(sc.y) : bf_hi(sc.x);
#pragma unroll
    for (int c = 0; c < 2; ++c) {
      const float sv = c ? s1 : s0;
      u32x4 o;
#pragma unroll
      for (int pp = 0; pp < 4; ++pp) o[pp] = pk_bf16(f[c][2 * pp] * sv, f[c][2 * pp + 1] * sv);
      if (lane < 56) *(LAS u32x4*)(scr + lane * 64 + (c0 + c) * 16) = o;
    }
  };
  auto publish = [&](LAS char* scr, int g) {
    const int so = nt * J_B + (g * 16 + mt) * KB_B;
#pragma unroll
    for (int s2 = 0; s2 < 4; ++s2) {
      const u32x4 v = *(const LAS u32x4*)(scr + s2 * 1024 + lane * 16);
      const int vo = ((s2 == 3 && lane >= 32) ? 0x7f000000 : lane * 16) + so + s2 * 1024;
      __builtin_amdgcn_raw_buffer_store_b128(v, rw, vo, 0, 16);
    }
  };
#define SELF_PF(g) if (SELFPF) __builtin_amdgcn_raw_ptr_buffer_load_lds(rw, (LAS void*)(smem + DUMMY_OFF), 4, lane < 28 ? lane * 128 : 0x7ffffff0, nt * J_B + ((g) * 16 + mt) * KB_B, 0, 16)
  u32x2 E, tag;
  unsigned cbv;

  if (w >= 6) {
    const int pw = w - 6;
    __builtin_amdgcn_s_setprio(3);
    LAS char* scr = smem + SCR_OFF + (4 + pw) * SCR_W;
    u32x2 sA, sB;
    E_LOAD();
    { const u16* pc = cb + (lane & 15); asm volatile("global_load_ushort %0, %1, off" : "=v"(cbv) : "v"(pc) : "memory"); }
    SCALE_LOAD(sA, 4 + pw);
    SCALE_LOAD(sB, 6 + pw);
    i32x4 qa[8], qb[8];
    wait_until(T0, pw ? T_G5 : T_G4);
    RAW_LOAD(qa, 4 + pw);
    wait_until(T0, pw ? T_G7 : T_G6);
    RAW_LOAD(qb, 6 + pw);
    WAIT_V(16);
    asm volatile("" : "+v"(E), "+v"(cbv), "+v"(sA), "+v"(sB));
    tag.x = E.x + 1u;
    tag.y = E.y ^ 0x5bd1e995u;
    if (lane < 16) *(LAS float*)(scr + 3584 + lane * 4) = __uint_as_float(cbv << 16);
    WAIT_V(8);
    STAMP(1);
    dequant2(qa, scr, sA, 0);
    dequant2(qa, scr, sA, 2);
    publish(scr, 4 + pw);
    STAMP(2);
    WAIT_V(4);
    STAMP(4);
    dequant2(qb, scr, sB, 0);
    dequant2(qb, scr, sB, 2);
    WAIT_V(0);
    FLAG_STORE(4 + pw);
    publish(scr, 6 + pw);
    STAMP(3);
#define PBAR(t) do { STAMP(9 + (t)); BAR(); } while (0)
    BAR();
    PBAR(0);
    WAIT_V(0);
    FLAG_STORE(6 + pw);
    PBAR(1); PBAR(2); PBAR(3); PBAR(4); PBAR(5); PBAR(6); PBAR(7);
    PBAR(8); PBAR(9); PBAR(10); PBAR(11); PBAR(12); PBAR(13); PBAR(14); PBAR(15);
  } else if (w >= 4) {
    const int lw = w - 4;
    const __amdgpu_buffer_rsrc_t rx = __builtin_amdgcn_make_buffer_rsrc((void*)x, 0, M_ * K_ * 2, 0x00020000);
    const int p = lane & 7, rl = lane >> 3, h = lane >> 4;
    int vx[2];
#pragma unroll
    for (int par = 0; par < 2; ++par) vx[par] = rl * (K_ * 2) + (p ^ (4 * par + h)) * 16;
    const int vw = lane * 16;
    const int sx0 = (m0 + lw * 64) * (K_ * 2);
    const int sw0 = nt * J_B + lw * 14 * 1024;
    auto issue_xwarm = [&](int piece) {
      __builtin_amdgcn_raw_ptr_buffer_load_lds(rx, (LAS void*)(smem + DUMMY_OFF), 4,
                                               (lane < 32 && piece >= 0) ? (lane >> 2) * (K_ * 2) + (lane & 3) * 128 : 0x7ffffff0,
                                               b * 8 * (K_ * 2) + (piece < 0 ? 0 : piece) * 512, 0, 0);
    };
    auto issue_x = [&](int kt, int slot) {
      LAS char* base = smem + slot * SLOT_B;
#pragma unroll
      for (int i = 0; i < 8; ++i)
        __builtin_amdgcn_raw_ptr_buffer_load_lds(rx, (LAS void*)(base + (lw * 8 + i) * 1024), 16, vx[i & 1], sx0 + i * 8 * (K_ * 2) + kt * 128, 0, 0);
    };
    auto issue_w = [&](int kt, int slot) {
      LAS char* base = smem + slot * SLOT_B;
#pragma unroll
      for (int i = 0; i < 14; ++i)
        __builtin_amdgcn_raw_ptr_buffer_load_lds(rw, (LAS void*)(base + XT_B + (lw * 14 + i) * 1024), 16, vw, sw0 + i * 1024 + kt * (8 * KB_B), 0, 16);
    };
    const char* pf = ws + FLAG_OFF + nt * 8 * 128 + (lane & 15) * 8;
    u32x2 Fa, Fb, Fs;
#define POLL_ISSUE(F, g) do { const char* pp_ = pf + (g) * 128; asm volatile("global_load_dwordx2 %0, %1, off sc1" : "=v"(F) : "v"(pp_) : "memory"); } while (0)
#define POLL_OK(F) (__builtin_amdgcn_ballot_w64(F.x == tag.x && F.y == tag.y) == ~0ull)
#define PIN(F) asm volatile("" : "+v"(F))
    E_LOAD();
    wait_until(T0, T_XWA);
    issue_xwarm(lw == 0 ? 0 : -1);
    WAIT_V(1);
    asm volatile("" : "+v"(E));
    tag.x = E.x + 1u;
    tag.y = E.y ^ 0x5bd1e995u;
    wait_until(T0, T_POLL);
    STAMP(1);
    auto spin = [&](int g) {
      const unsigned long long t0 = __builtin_amdgcn_s_memrealtime();
      for (;;) {
        POLL_ISSUE(Fs, g);
        WAIT_V(0);
        PIN(Fs);
        if (POLL_OK(Fs)) break;
        if (__builtin_amdgcn_s_memrealtime() - t0 > 2000000ull) break;
        __builtin_amdgcn_s_sleep(1);
      }
    };
    spin(0);
    STAMP(2);
    issue_x(0, 0);
    issue_w(0, 0);
    issue_x(1, 1);
    issue_w(1, 1);
    POLL_ISSUE(Fa, 1);
    POLL_ISSUE(Fb, 2);
    issue_xwarm(lw == 0 ? 1 : -1);
    issue_xwarm(2 + lw);
    WAIT_V(26);
    STAMP(3);
    BAR();
    WAIT_V(2);
    PIN(Fa); PIN(Fb);
    if (!POLL_OK(Fa)) spin(1);
    int slot = 2;
#define TRIP(j, FNEW, FOLD, W1) do { \
      issue_x(2 * (j) + 2, slot); \
      issue_w(2 * (j) + 2, slot); \
      slot = slot == 2 ? 0 : slot + 1; \
      WAIT_V(W1); \
      STAMP(9 + 2 * (j)); \
      BAR(); \
      issue_x(2 * (j) + 3, slot); \
      issue_w(2 * (j) + 3, slot); \
      slot = slot == 2 ? 0 : slot + 1; \
      POLL_ISSUE(FNEW, (j) + 3 < 8 ? (j) + 3 : 7); \
      WAIT_V(23); \
      PIN(FOLD); \
      const bool ok_ = POLL_OK(FOLD); \
      STAMP(10 + 2 * (j)); \
      BAR(); \
      if (!ok_ && (j) + 2 < 8) spin((j) + 2); \
    } while (0)
    TRIP(0, Fa, Fb, 24);
#pragma unroll 1
    for (int j = 1; j < 7; j += 2) {
      TRIP(j, Fb, Fa, 23);
      if (j == 5 && mt == 0 && lw == 0 && lane == 0) {
        const u32x2 ne = {E.x + 1u, E.y};
        asm volatile("global_store_dwordx2 %0, %1, off sc1" ::"v"(pe), "v"(ne) : "memory");
      }
      TRIP(j + 1, Fa, Fb, 23);
    }
    WAIT_V(0);
    BAR();
    BAR();
  } else {
    const int wm = w & 1, wn = w >> 1;
    {
      LAS char* scr = smem + SCR_OFF + w * SCR_W;
      u32x2 sA;
      i32x4 q[8];
      E_LOAD();
      { const u16* pc = cb + (lane & 15); asm volatile("global_load_ushort %0, %1, off" : "=v"(cbv) : "v"(pc) : "memory"); }
      SCALE_LOAD(sA, w);
      if (w) wait_until(T0, w == 1 ? T_G1 : w == 2 ? T_G2 : T_G3);
      RAW_LOAD(q, w);
      WAIT_V(8);
      asm volatile("" : "+v"(E), "+v"(cbv), "+v"(sA));
      tag.x = E.x + 1u;
      tag.y = E.y ^ 0x5bd1e995u;
      if (lane < 16) *(LAS float*)(scr + 3584 + lane * 4) = __uint_as_float(cbv << 16);
      WAIT_V(0);
      STAMP(1);
      dequant2(q, scr, sA, 0);
      dequant2(q, scr, sA, 2);
      publish(scr, w);
      STAMP(2);
      WAIT_V(0);
      FLAG_STORE(w);
      SELF_PF(w);
      STAMP(3);
    }
    const int fr = lane & 15, fh = lane >> 4;
    const int xa0 = (wm * 64 + fr) * 128 + (fh ^ (fr >> 1)) * 16;
    const int wa0 = XT_B + fh * KB_B + (wn * 112 + fr) * 16;
    f32x4 acc[4][7];
#pragma unroll
    for (int mi = 0; mi < 4; ++mi)
#pragma unroll
      for (int ni = 0; ni < 7; ++ni) acc[mi][ni] = (f32x4){0.f, 0.f, 0.f, 0.f};
    bf16x8 X0[4], W0[7], X1[4], W1[7];
#define RD(XF, WF, so, ks) do { \
    _Pragma("unroll") for (int ni = 0; ni < 7; ++ni) WF[ni] = *(const LAS bf16x8*)(smem + (so) + wa0 + (ks) * 4 * KB_B + ni * 256); \
    _Pragma("unroll") for (int mi = 0; mi < 4; ++mi) XF[mi] = *(const LAS bf16x8*)(smem + (so) + ((xa0 ^ ((ks) * 64))) + mi * 2048); } while (0)
#define MM(XF, WF) do { \
    _Pragma("unroll") for (int mi = 0; mi < 4; ++mi) _Pragma("unroll") for (int ni = 0; ni < 7; ++ni) \
      acc[mi][ni] = __builtin_amdgcn_mfma_f32_16x16x32_bf16(WF[ni], XF[mi], acc[mi][ni], 0, 0, 0); } while (0)
#define ILV() do { _Pragma("unroll") for (int g_ = 0; g_ < 11; ++g_) { \
      __builtin_amdgcn_sched_group_barrier(0x100, 1, 0); __builtin_amdgcn_sched_group_barrier(0x8, 2, 0); } \
      __builtin_amdgcn_sched_group_barrier(0x8, 6, 0); } while (0)
    STAMP(4);
    BAR();
    STAMP(8);
    CSTAMP(26);
    int so = 0;
    RD(X0, W0, so, 0);
#pragma unroll 1
    for (int t = 0; t < 15; ++t) {
      WAIT_L0();
      __builtin_amdgcn_sched_barrier(0);
      RD(X1, W1, so, 1);
      MM(X0, W0);
      ILV();
      __builtin_amdgcn_sched_barrier(0);
      WAIT_L0();
      STAMP(9 + t);
      BAR();
      __builtin_amdgcn_sched_barrier(0);
      so = so == 2 * SLOT_B ? 0 : so + SLOT_B;
      RD(X0, W0, so, 0);
      MM(X1, W1);
      ILV();
      __builtin_amdgcn_sched_barrier(0);
    }
    WAIT_L0();
    __builtin_amdgcn_sched_barrier(0);
    RD(X1, W1, so, 1);
    MM(X0, W0);
    ILV();
    __builtin_amdgcn_sched_barrier(0);
    WAIT_L0();
    STAMP(24);
    BAR();
    MM(X1, W1);
    STAMP(5);
    CSTAMP(27);
#pragma unroll
    for (int mi = 0; mi < 4; ++mi)
#pragma unroll
      for (int ni = 0; ni < 7; ++ni) {
        u32x2 o = {pk_bf16(acc[mi][ni][0], acc[mi][ni][1]), pk_bf16(acc[mi][ni][2], acc[mi][ni][3])};
        *(LAS u32x2*)(smem + (wm * 64 + mi * 16 + fr) * OPITCH + (wn * 112 + ni * 16 + fh * 4) * 2) = o;
      }
  }
  WAIT_L0();
  BAR();
#pragma unroll
  for (int r = 0; r < 7; ++r) {
    const int c = r * 512 + tid;
    const int row = c / 28, ch = c - row * 28;
    const u32x4 v = *(const LAS u32x4*)(smem + row * OPITCH + ch * 16);
    __builtin_nontemporal_store(v, (u32x4*)((char*)out + (size_t)(m0 + row) * (N_ * 2) + n0 * 2 + ch * 16));
  }
  STAMP(6);
}

extern "C" void kernel_launch(void* const* d_in, const int* in_sizes, int n_in, void* d_out, int out_size, void* d_ws, size_t ws_size,
                              hipStream_t stream) {
  const u16* x = (const u16*)d_in[0];
  const int* qw = (const int*)d_in[1];
  const u16* cb = (const u16*)d_in[2];
  const u16* am = (const u16*)d_in[3];
  (void)hipFuncSetAttribute((const void*)k_fused, hipFuncAttributeMaxDynamicSharedMemorySize, LDS_B);
  k_fused<<<256, 512, LDS_B, stream>>>(x, qw, cb, am, (char*)d_ws, (u16*)d_out);
}
```

```cpp
#define OAUX 16
#include <hip/hip_runtime.h>
#include <stdint.h>
typedef unsigned short u16;
typedef __attribute__((ext_vector_type(8))) __bf16 bf16x8;
typedef __attribute__((ext_vector_type(2))) __bf16 bf16x2;
typedef float f32x4 __attribute__((ext_vector_type(4)));
typedef float f32x2 __attribute__((ext_vector_type(2)));
typedef int i32x4 __attribute__((ext_vector_type(4)));
typedef unsigned u32x4 __attribute__((ext_vector_type(4)));
typedef unsigned u32x2 __attribute__((ext_vector_type(2)));
#define LAS __attribute__((address_space(3)))

constexpr int M_ = 2048, N_ = 3584, K_ = 1024;
constexpr int BM = 128, BN = 224;
constexpr int XT_B = BM * 64 * 2;
constexpr int WT_B = BN * 64 * 2;
constexpr int SLOT_B = XT_B + WT_B;
constexpr int NSLOT = 3;
constexpr int KB_B = BN * 16;
constexpr int J_B = (K_ / 8) * KB_B;
constexpr int W_TOTAL = (N_ / BN) * J_B;
constexpr int FLAG_OFF = W_TOTAL;
constexpr int EPOCH_OFF = FLAG_OFF + 128 * 128;
constexpr int SCR_OFF = NSLOT * SLOT_B;
constexpr int SCR_W = 3712;
constexpr int DUMMY_OFF = SCR_OFF + 6 * SCR_W;
constexpr int SIG_OFF = DUMMY_OFF + 256;
constexpr int CNT_OFF = SIG_OFF + 16;
constexpr int LDS_B = CNT_OFF + 16;
constexpr int OPITCH = 464;

#define T_G1 45
#define T_G2 70
#define T_G3 95
#define T_G4 60
#define T_G5 85
#define T_G6 220
#define T_G7 250
#define T_XWA 25
#define T_XWB 60
#define T_POLL 200
#define EPI 2
#define XWPOS 1
#define SELFPF 0

__device__ __forceinline__ unsigned pk_bf16(float a, float b) {
  f32x2 v = {a, b};
  bf16x2 r = __builtin_convertvector(v, bf16x2);
  return __builtin_bit_cast(unsigned, r);
}
__device__ __forceinline__ float bf_lo(unsigned u) { return __uint_as_float(u << 16); }
__device__ __forceinline__ float bf_hi(unsigned u) { return __uint_as_float(u & 0xffff0000u); }

#define WAIT_V(n) asm volatile("s_waitcnt vmcnt(%0)" ::"n"(n) : "memory")
#define WAIT_L0() __builtin_amdgcn_s_waitcnt(0xC07F)
#define BAR() do { asm volatile("" ::: "memory"); __builtin_amdgcn_s_barrier(); asm volatile("" ::: "memory"); } while (0)

#define STAMP(i)
#define CSTAMP(i)
#define DIAG_ARG

__device__ __forceinline__ void wait_until(unsigned long long t0, unsigned ticks) {
  while (__builtin_amdgcn_s_memrealtime() - t0 < ticks) __builtin_amdgcn_s_sleep(1);
}

__global__ __launch_bounds__(512, 2) void k_fused(const u16* __restrict__ x, const int* __restrict__ qw, const u16* __restrict__ cb,
                                                  const u16* __restrict__ am, char* __restrict__ ws, u16* __restrict__ out DIAG_ARG) {
  extern __shared__ __attribute__((aligned(16))) char smem_g[];
  LAS char* smem = (LAS char*)smem_g;
  const unsigned long long T0 = __builtin_amdgcn_s_memrealtime();
  const int tid = threadIdx.x, lane = tid & 63;
  const int w = __builtin_amdgcn_readfirstlane(tid >> 6);
  const int b = blockIdx.x, xcd = b & 7, bi = b >> 3;
#define MAP 0
  const int mt = MAP ? (bi & 15) : (xcd & 1) * 8 + (bi & 7), nt = MAP ? xcd * 2 + (bi >> 4) : (xcd >> 1) * 4 + (bi >> 3);
  const int m0 = mt * BM, n0 = nt * BN;
  STAMP(0);
  const __amdgpu_buffer_rsrc_t rw = __builtin_amdgcn_make_buffer_rsrc((void*)ws, 0, EPOCH_OFF + 16 * 128, 0x00020000);
  const char* pe = ws + EPOCH_OFF + nt * 128;
  char* pfl = ws + FLAG_OFF + nt * 8 * 128 + mt * 8;

  const int cbk = lane < 56 ? lane : 55;
  const int* qsrc = qw + (size_t)(mt * 8) * N_ + n0 + cbk * 4;
#define RAW_LOAD(q, g) do { const int* s_ = qsrc + (size_t)(g) * 128 * N_; _Pragma("unroll") for (int r = 0; r < 8; ++r) { const int* p_ = s_ + (size_t)r * N_; \
      asm volatile("global_load_dwordx4 %0, %1, off nt" : "=v"(q[r]) : "v"(p_) : "memory"); } } while (0)
#define SCALE_LOAD(dst, g) do { const u16* p_ = am + (size_t)(g) * N_ + n0 + cbk * 4; asm volatile("global_load_dwordx2 %0, %1, off" : "=v"(dst) : "v"(p_) : "memory"); } while (0)
#define E_LOAD() asm volatile("global_load_dwordx2 %0, %1, off sc1" : "=v"(E) : "v"(pe) : "memory")
#define FLAG_STORE(g) do { char* p_ = pfl + (g) * 128; asm volatile("global_store_dwordx2 %0, %1, off sc1" ::"v"(p_), "v"(tag) : "memory"); } while (0)
  auto dequant2 = [&](i32x4 (&q)[8], LAS char* scr, const u32x2 sc, int c0) {
#pragma unroll
    for (int r = 0; r < 8; ++r) asm volatile("" : "+v"(q[r]));
    const LAS float* lut = (const LAS float*)(scr + 3584);
    float f[2][8];
#pragma unroll
    for (int c = 0; c < 2; ++c)
#pragma unroll
      for (int r = 0; r < 8; ++r) f[c][r] = lut[q[r][c0 + c]];
    __builtin_amdgcn_sched_group_barrier(0x2, 16, 0);
    __builtin_amdgcn_sched_group_barrier(0x100, 16, 0);
    const float s0 = c0 ? bf_lo(sc.y) : bf_lo(sc.x), s1 = c0 ? bf_hi(sc.y) : bf_hi(sc.x);
#pragma unroll
    for (int c = 0; c < 2; ++c) {
      const float sv = c ? s1 : s0;
      u32x4 o;
#pragma unroll
      for (int pp = 0; pp < 4; ++pp) o[pp] = pk_bf16(f[c][2 * pp] * sv, f[c][2 * pp + 1] * sv);
      if (lane < 56) *(LAS u32x4*)(scr + lane * 64 + (c0 + c) * 16) = o;
    }
  };
  auto publish = [&](LAS char* scr, int g) {
    const int so = nt * J_B + (g * 16 + mt) * KB_B;
#pragma unroll
    for (int s2 = 0; s2 < 4; ++s2) {
      const u32x4 v = *(const LAS u32x4*)(scr + s2 * 1024 + lane * 16);
      const int vo = ((s2 == 3 && lane >= 32) ? 0x7f000000 : lane * 16) + so + s2 * 1024;
      __builtin_amdgcn_raw_buffer_store_b128(v, rw, vo, 0, 16);
    }
  };
#define SELF_PF(g) if (SELFPF) __builtin_amdgcn_raw_ptr_buffer_load_lds(rw, (LAS void*)(smem + DUMMY_OFF), 4, lane < 28 ? lane * 128 : 0x7ffffff0, nt * J_B + ((g) * 16 + mt) * KB_B, 0, 16)
  u32x2 E, tag;
  unsigned cbv;

  if (w >= 6) {
    const int pw = w - 6;
    __builtin_amdgcn_s_setprio(3);
    LAS char* scr = smem + SCR_OFF + (4 + pw) * SCR_W;
    u32x2 sA, sB;
    E_LOAD();
    { const u16* pc = cb + (lane & 15); asm volatile("global_load_ushort %0, %1, off" : "=v"(cbv) : "v"(pc) : "memory"); }
    SCALE_LOAD(sA, 4 + pw);
    SCALE_LOAD(sB, 6 + pw);
    i32x4 qa[8], qb[8];
    wait_until(T0, pw ? T_G5 : T_G4);
    RAW_LOAD(qa, 4 + pw);
    wait_until(T0, pw ? T_G7 : T_G6);
    RAW_LOAD(qb, 6 + pw);
    WAIT_V(16);
    asm volatile("" : "+v"(E), "+v"(cbv), "+v"(sA), "+v"(sB));
    tag.x = E.x + 1u;
    tag.y = E.y ^ 0x5bd1e995u;
    if (lane < 16) *(LAS float*)(scr + 3584 + lane * 4) = __uint_as_float(cbv << 16);
    WAIT_V(8);
    STAMP(1);
    dequant2(qa, scr, sA, 0);
    dequant2(qa, scr, sA, 2);
    publish(scr, 4 + pw);
    STAMP(2);
    WAIT_V(4);
    STAMP(4);
    dequant2(qb, scr, sB, 0);
    dequant2(qb, scr, sB, 2);
    WAIT_V(0);
    FLAG_STORE(4 + pw);
    SELF_PF(4 + pw);
    publish(scr, 6 + pw);
    STAMP(3);
#define PBAR(t) do { STAMP(9 + (t)); BAR(); if ((t) == 4) CSTAMP(28); if ((t) == 14) CSTAMP(29); } while (0)
    BAR();
    PBAR(0);
    WAIT_V(0);
    FLAG_STORE(6 + pw);
    SELF_PF(6 + pw);
    PBAR(1); PBAR(2); PBAR(3); PBAR(4); PBAR(5); PBAR(6); PBAR(7);
    PBAR(8); PBAR(9); PBAR(10); PBAR(11); PBAR(12); PBAR(13); PBAR(14); PBAR(15);
  } else if (w >= 4) {
    const int lw = w - 4;
    const __amdgpu_buffer_rsrc_t rx = __builtin_amdgcn_make_buffer_rsrc((void*)x, 0, M_ * K_ * 2, 0x00020000);
    const int p = lane & 7, rl = lane >> 3, h = lane >> 4;
    int vx[2];
#pragma unroll
    for (int par = 0; par < 2; ++par) vx[par] = rl * (K_ * 2) + (p ^ (4 * par + h)) * 16;
    const int vw = lane * 16;
    const int sx0 = (m0 + lw * 64) * (K_ * 2);
    const int sw0 = nt * J_B + lw * 14 * 1024;
    auto issue_xwarm = [&](int piece) {
      __builtin_amdgcn_raw_ptr_buffer_load_lds(rx, (LAS void*)(smem + DUMMY_OFF), 4,
                                               (lane < 32 && piece >= 0) ? (lane >> 2) * (K_ * 2) + (lane & 3) * 128 : 0x7ffffff0,
                                               b * 8 * (K_ * 2) + (piece < 0 ? 0 : piece) * 512, 0, 0);
    };
    auto issue_x = [&](int kt, int slot) {
      LAS char* base = smem + slot * SLOT_B;
#pragma unroll
      for (int i = 0; i < 8; ++i)
        __builtin_amdgcn_raw_ptr_buffer_load_lds(rx, (LAS void*)(base + (lw * 8 + i) * 1024), 16, vx[i & 1], sx0 + i * 8 * (K_ * 2) + kt * 128, 0, 0);
    };
    auto issue_w = [&](int kt, int slot) {
      LAS char* base = smem + slot * SLOT_B;
#pragma unroll
      for (int i = 0; i < 14; ++i)
        __builtin_amdgcn_raw_ptr_buffer_load_lds(rw, (LAS void*)(base + XT_B + (lw * 14 + i) * 1024), 16, vw, sw0 + i * 1024 + kt * (8 * KB_B), 0, 16);
    };
    const char* pf = ws + FLAG_OFF + nt * 8 * 128 + (lane & 15) * 8;
    u32x2 Fa, Fb, Fs;
#define POLL_ISSUE(F, g) do { const char* pp_ = pf + (g) * 128; asm volatile("global_load_dwordx2 %0, %1, off sc1" : "=v"(F) : "v"(pp_) : "memory"); } while (0)
#define POLL_OK(F) (__builtin_amdgcn_ballot_w64(F.x == tag.x && F.y == tag.y) == ~0ull)
#define PIN(F) asm volatile("" : "+v"(F))
    if (lw == 0) *(volatile LAS unsigned*)(smem + CNT_OFF) = 0u;
    E_LOAD();
    wait_until(T0, T_XWA);
    issue_xwarm(lw == 0 ? 0 : -1);
    WAIT_V(1);
    asm volatile("" : "+v"(E));
    tag.x = E.x + 1u;
    tag.y = E.y ^ 0x5bd1e995u;
    wait_until(T0, T_POLL);
    STAMP(1);
    auto spin = [&](int g) {
      const unsigned long long t0 = __builtin_amdgcn_s_memrealtime();
      for (;;) {
        POLL_ISSUE(Fs, g);
        WAIT_V(0);
        PIN(Fs);
        if (POLL_OK(Fs)) break;
        if (__builtin_amdgcn_s_memrealtime() - t0 > 2000000ull) break;
        __builtin_amdgcn_s_sleep(1);
      }
    };
    spin(0);
    STAMP(2);
    issue_x(0, 0);
    issue_w(0, 0);
    issue_x(1, 1);
    issue_w(1, 1);
    POLL_ISSUE(Fa, 1);
    POLL_ISSUE(Fb, 2);
    if (XWPOS == 0) { issue_xwarm(lw == 0 ? 1 : -1); issue_xwarm(2 + lw); }
    if (XWPOS == 0) WAIT_V(26); else WAIT_V(24);
    STAMP(3);
    BAR();
    if (XWPOS == 0) WAIT_V(2); else WAIT_V(0);
    PIN(Fa); PIN(Fb);
    if (!POLL_OK(Fa)) spin(1);
    int slot = 2;
#define TRIP(j, FNEW, FOLD, W1, XW, W2) do { \
      issue_x(2 * (j) + 2, slot); \
      issue_w(2 * (j) + 2, slot); \
      slot = slot == 2 ? 0 : slot + 1; \
      if (XW) { issue_xwarm(lw == 0 ? 1 : -1); issue_xwarm(2 + lw); } \
      WAIT_V(W1); \
      STAMP(9 + 2 * (j)); \
      BAR(); \
      issue_x(2 * (j) + 3, slot); \
      issue_w(2 * (j) + 3, slot); \
      slot = slot == 2 ? 0 : slot + 1; \
      POLL_ISSUE(FNEW, (j) + 3 < 8 ? (j) + 3 : 7); \
      WAIT_V(W2); \
      PIN(FOLD); \
      const bool ok_ = POLL_OK(FOLD); \
      STAMP(10 + 2 * (j)); \
      BAR(); \
      if (!ok_ && (j) + 2 < 8) spin((j) + 2); \
    } while (0)
    if (XWPOS == 0) TRIP(0, Fa, Fb, 24, 0, 23); else TRIP(0, Fa, Fb, 24, 1, 25);
#pragma unroll 1
    for (int j = 1; j < 7; j += 2) {
      TRIP(j, Fb, Fa, 23, 0, 23);
      if (j == 5 && mt == 0 && lw == 0 && lane == 0) {
        const u32x2 ne = {E.x + 1u, E.y};
        asm volatile("global_store_dwordx2 %0, %1, off sc1" ::"v"(pe), "v"(ne) : "memory");
      }
      TRIP(j + 1, Fa, Fb, 23, 0, 23);
    }
    WAIT_V(0);
    BAR();
    BAR();
  } else {
    const int wm = w & 1, wn = w >> 1;
    {
      LAS char* scr = smem + SCR_OFF + w * SCR_W;
      u32x2 sA;
      i32x4 q[8];
      E_LOAD();
      { const u16* pc = cb + (lane & 15); asm volatile("global_load_ushort %0, %1, off" : "=v"(cbv) : "v"(pc) : "memory"); }
      SCALE_LOAD(sA, w);
      if (w) wait_until(T0, w == 1 ? T_G1 : w == 2 ? T_G2 : T_G3);
      RAW_LOAD(q, w);
      WAIT_V(8);
      asm volatile("" : "+v"(E), "+v"(cbv), "+v"(sA));
      tag.x = E.x + 1u;
      tag.y = E.y ^ 0x5bd1e995u;
      if (lane < 16) *(LAS float*)(scr + 3584 + lane * 4) = __uint_as_float(cbv << 16);
      WAIT_V(0);
      STAMP(1);
      dequant2(q, scr, sA, 0);
      dequant2(q, scr, sA, 2);
      publish(scr, w);
      STAMP(2);
      WAIT_V(0);
      FLAG_STORE(w);
      SELF_PF(w);
      STAMP(3);
    }
    const int fr = lane & 15, fh = lane >> 4;
    const int xa0 = (wm * 64 + fr) * 128 + (fh ^ (fr >> 1)) * 16;
    const int wa0 = XT_B + fh * KB_B + (wn * 112 + fr) * 16;
    f32x4 acc[4][7];
#pragma unroll
    for (int mi = 0; mi < 4; ++mi)
#pragma unroll
      for (int ni = 0; ni < 7; ++ni) acc[mi][ni] = (f32x4){0.f, 0.f, 0.f, 0.f};
    bf16x8 X0[4], W0[7], X1[4], W1[7];
#define RD(XF, WF, so, ks) do { \
    _Pragma("unroll") for (int ni = 0; ni < 7; ++ni) WF[ni] = *(const LAS bf16x8*)(smem + (so) + wa0 + (ks) * 4 * KB_B + ni * 256); \
    _Pragma("unroll") for (int mi = 0; mi < 4; ++mi) XF[mi] = *(const LAS bf16x8*)(smem + (so) + ((xa0 ^ ((ks) * 64))) + mi * 2048); } while (0)
#define MM(XF, WF) do { \
    _Pragma("unroll") for (int mi = 0; mi < 4; ++mi) _Pragma("unroll") for (int ni = 0; ni < 7; ++ni) \
      acc[mi][ni] = __builtin_amdgcn_mfma_f32_16x16x32_bf16(WF[ni], XF[mi], acc[mi][ni], 0, 0, 0); } while (0)
#define ILV() do { _Pragma("unroll") for (int g_ = 0; g_ < 11; ++g_) { \
      __builtin_amdgcn_sched_group_barrier(0x100, 1, 0); __builtin_amdgcn_sched_group_barrier(0x8, 2, 0); } \
      __builtin_amdgcn_sched_group_barrier(0x8, 6, 0); } while (0)
    STAMP(4);
    BAR();
    STAMP(8);
    CSTAMP(26);
    int so = 0;
    RD(X0, W0, so, 0);
#pragma unroll 1
    for (int t = 0; t < 15; ++t) {
      WAIT_L0();
      __builtin_amdgcn_sched_barrier(0);
      RD(X1, W1, so, 1);
      MM(X0, W0);
      ILV();
      __builtin_amdgcn_sched_barrier(0);
      WAIT_L0();
      STAMP(9 + t);
      BAR();
      __builtin_amdgcn_sched_barrier(0);
      so = so == 2 * SLOT_B ? 0 : so + SLOT_B;
      RD(X0, W0, so, 0);
      MM(X1, W1);
      ILV();
      __builtin_amdgcn_sched_barrier(0);
    }
    WAIT_L0();
    __builtin_amdgcn_sched_barrier(0);
    RD(X1, W1, so, 1);
    MM(X0, W0);
    ILV();
    __builtin_amdgcn_sched_barrier(0);
    WAIT_L0();
    STAMP(24);
    BAR();
#define MMROW(mi) do { _Pragma("unroll") for (int ni = 0; ni < 7; ++ni) \
      acc[mi][ni] = __builtin_amdgcn_mfma_f32_16x16x32_bf16(W1[ni], X1[mi], acc[mi][ni], 0, 0, 0); } while (0)
#define CVTROW(mi) do { _Pragma("unroll") for (int ni = 0; ni < 7; ++ni) { \
        const u32x2 o_ = {pk_bf16(acc[mi][ni][0], acc[mi][ni][1]), pk_bf16(acc[mi][ni][2], acc[mi][ni][3])}; \
        *(LAS u32x2*)(smem + (wm * 64 + (mi) * 16 + fr) * OPITCH + (wn * 112 + ni * 16 + fh * 4) * 2) = o_; } \
      __builtin_amdgcn_sched_barrier(0); \
      WAIT_L0(); \
      if (lane == 0) __hip_atomic_fetch_add((LAS unsigned*)(smem + CNT_OFF), 1u << (8 * (mi)), __ATOMIC_RELAXED, __HIP_MEMORY_SCOPE_WORKGROUP); } while (0)
    MMROW(0);
    MMROW(1);
    CVTROW(0);
    MMROW(2);
    CVTROW(1);
    MMROW(3);
    CVTROW(2);
    STAMP(5);
    CSTAMP(27);
    CVTROW(3);
    STAMP(6);
  }
  if (w >= 4) {
    const __amdgpu_buffer_rsrc_t ro = __builtin_amdgcn_make_buffer_rsrc((void*)out, 0, M_ * N_ * 2, 0x00020000);
    const int hh = w - 4;
    int lo_[4], go_[4];
#pragma unroll
    for (int i = 0; i < 4; ++i) {
      const int k = hh + 4 * i, c = k * 64 + lane, row32 = c / 28, ch = c - row32 * 28;
      const int row = (row32 >> 4) * 64 + (row32 & 15);
      lo_[i] = k < 14 ? row * OPITCH + ch * 16 : 0;
      go_[i] = k < 14 ? (m0 + row) * (N_ * 2) + n0 * 2 + ch * 16 : 0x7f000000;
    }
#pragma unroll
    for (int r = 0; r < 4; ++r) {
      while (((*(volatile LAS unsigned*)(smem + CNT_OFF) >> (8 * r)) & 0xffu) < 4u) __builtin_amdgcn_s_sleep(0);
      asm volatile("" ::: "memory");
      u32x4 v[4];
#pragma unroll
      for (int i = 0; i < 4; ++i) v[i] = *(const LAS u32x4*)(smem + lo_[i] + r * 16 * OPITCH);
#pragma unroll
      for (int i = 0; i < 4; ++i) __builtin_amdgcn_raw_buffer_store_b128(v[i], ro, go_[i] + (go_[i] < 0x7f000000 ? r * 16 * (N_ * 2) : 0), 0, OAUX);
    }
    STAMP(6);
  }
}

extern "C" void kernel_launch(void* const* d_in, const int* in_sizes, int n_in, void* d_out, int out_size, void* d_ws, size_t ws_size,
                              hipStream_t stream) {
  const u16* x = (const u16*)d_in[0];
  const int* qw = (const int*)d_in[1];
  const u16* cb = (const u16*)d_in[2];
  const u16* am = (const u16*)d_in[3];
  (void)hipFuncSetAttribute((const void*)k_fused, hipFuncAttributeMaxDynamicSharedMemorySize, LDS_B);
  k_fused<<<256, 512, LDS_B, stream>>>(x, qw, cb, am, (char*)d_ws, (u16*)d_out);
}
```

```cpp
#include <hip/hip_runtime.h>
#include <stdint.h>
typedef unsigned short u16;
typedef __attribute__((ext_vector_type(8))) __bf16 bf16x8;
typedef __attribute__((ext_vector_type(2))) __bf16 bf16x2;
typedef float f32x4 __attribute__((ext_vector_type(4)));
typedef float f32x2 __attribute__((ext_vector_type(2)));
typedef int i32x4 __attribute__((ext_vector_type(4)));
typedef unsigned u32x4 __attribute__((ext_vector_type(4)));
typedef unsigned u32x2 __attribute__((ext_vector_type(2)));
#define LAS __attribute__((address_space(3)))

constexpr int M_ = 2048, N_ = 3584, K_ = 1024;
constexpr int BM = 128, BN = 224;
constexpr int XT_B = BM * 64 * 2;
constexpr int WT_B = BN * 64 * 2;
constexpr int SLOT_B = XT_B + WT_B;
constexpr int NSLOT = 3;
constexpr int KB_B = BN * 16;
constexpr int J_B = (K_ / 8) * KB_B;
constexpr int W_TOTAL = (N_ / BN) * J_B;
constexpr int FLAG_OFF = W_TOTAL;
constexpr int EPOCH_OFF = FLAG_OFF + 128 * 128;
constexpr int SCR_OFF = NSLOT * SLOT_B;
constexpr int SCR_W = 3712;
constexpr int DUMMY_OFF = SCR_OFF + 6 * SCR_W;
constexpr int SIG_OFF = DUMMY_OFF + 256;
constexpr int CNT_OFF = SIG_OFF + 16;
constexpr int LDS_B = CNT_OFF + 16;
constexpr int OPITCH = 464;

#define T_G1 45
#define T_G2 70
#define T_G3 95
#define T_G4 60
#define T_G5 85
#define T_G6 220
#define T_G7 250
#define T_XWA 25
#define T_XWB 60
#define T_POLL 200
#define EPI 2
#define OAUX 2
#define XWPOS 1
#define TLBW 0
#define SELFPF 0

__device__ __forceinline__ unsigned pk_bf16(float a, float b) {
  f32x2 v = {a, b};
  bf16x2 r = __builtin_convertvector(v, bf16x2);
  return __builtin_bit_cast(unsigned, r);
}
__device__ __forceinline__ float bf_lo(unsigned u) { return __uint_as_float(u << 16); }
__device__ __forceinline__ float bf_hi(unsigned u) { return __uint_as_float(u & 0xffff0000u); }

#define WAIT_V(n) asm volatile("s_waitcnt vmcnt(%0)" ::"n"(n) : "memory")
#define WAIT_L0() __builtin_amdgcn_s_waitcnt(0xC07F)
#define BAR() do { asm volatile("" ::: "memory"); __builtin_amdgcn_s_barrier(); asm volatile("" ::: "memory"); } while (0)

#define STAMP(i)
#define CSTAMP(i)
#define DIAG_ARG

__device__ __forceinline__ void wait_until(unsigned long long t0, unsigned ticks) {
  while (__builtin_amdgcn_s_memrealtime() - t0 < ticks) __builtin_amdgcn_s_sleep(1);
}

__global__ __launch_bounds__(512, 2) void k_fused(const u16* __restrict__ x, const int* __restrict__ qw, const u16* __restrict__ cb,
                                                  const u16* __restrict__ am, char* __restrict__ ws, u16* __restrict__ out DIAG_ARG) {
  extern __shared__ __attribute__((aligned(16))) char smem_g[];
  LAS char* smem = (LAS char*)smem_g;
  const unsigned long long T0 = __builtin_amdgcn_s_memrealtime();
  const int tid = threadIdx.x, lane = tid & 63;
  const int w = __builtin_amdgcn_readfirstlane(tid >> 6);
  const int b = blockIdx.x, xcd = b & 7, bi = b >> 3;
#define MAP 0
  const int mt = MAP ? (bi & 15) : (xcd & 1) * 8 + (bi & 7), nt = MAP ? xcd * 2 + (bi >> 4) : (xcd >> 1) * 4 + (bi >> 3);
  const int m0 = mt * BM, n0 = nt * BN;
  STAMP(0);
  const __amdgpu_buffer_rsrc_t rw = __builtin_amdgcn_make_buffer_rsrc((void*)ws, 0, EPOCH_OFF + 16 * 128, 0x00020000);
  const char* pe = ws + EPOCH_OFF + nt * 128;
  char* pfl = ws + FLAG_OFF + nt * 8 * 128 + mt * 8;

  const int cbk = lane < 56 ? lane : 55;
  const int* qsrc = qw + (size_t)(mt * 8) * N_ + n0 + cbk * 4;
#define RAW_LOAD(q, g) do { const int* s_ = qsrc + (size_t)(g) * 128 * N_; _Pragma("unroll") for (int r = 0; r < 8; ++r) { const int* p_ = s_ + (size_t)r * N_; \
      asm volatile("global_load_dwordx4 %0, %1, off nt" : "=v"(q[r]) : "v"(p_) : "memory"); } } while (0)
#define SCALE_LOAD(dst, g) do { const u16* p_ = am + (size_t)(g) * N_ + n0 + cbk * 4; asm volatile("global_load_dwordx2 %0, %1, off" : "=v"(dst) : "v"(p_) : "memory"); } while (0)
#define E_LOAD() asm volatile("global_load_dwordx2 %0, %1, off sc1" : "=v"(E) : "v"(pe) : "memory")
#define FLAG_STORE(g) do { char* p_ = pfl + (g) * 128; asm volatile("global_store_dwordx2 %0, %1, off sc1" ::"v"(p_), "v"(tag) : "memory"); } while (0)
  auto dequant2 = [&](i32x4 (&q)[8], LAS char* scr, const u32x2 sc, int c0) {
#pragma unroll
    for (int r = 0; r < 8; ++r) asm volatile("" : "+v"(q[r]));
    const LAS float* lut = (const LAS float*)(scr + 3584);
    float f[2][8];
#pragma unroll
    for (int c = 0; c < 2; ++c)
#pragma unroll
      for (int r = 0; r < 8; ++r) f[c][r] = lut[q[r][c0 + c]];
    __builtin_amdgcn_sched_group_barrier(0x2, 16, 0);
    __builtin_amdgcn_sched_group_barrier(0x100, 16, 0);
    const float s0 = c0 ? bf_lo(sc.y) : bf_lo(sc.x), s1 = c0 ? bf_hi(sc.y) : bf_hi(sc.x);
#pragma unroll
    for (int c = 0; c < 2; ++c) {
      const float sv = c ? s1 : s0;
      u32x4 o;
#pragma unroll
      for (int pp = 0; pp < 4; ++pp) o[pp] = pk_bf16(f[c][2 * pp] * sv, f[c][2 * pp + 1] * sv);
      if (lane < 56) *(LAS u32x4*)(scr + lane * 64 + (c0 + c) * 16) = o;
    }
  };
  auto publish = [&](LAS char* scr, int g) {
    const int so = nt * J_B + (g * 16 + mt) * KB_B;
#pragma unroll
    for (int s2 = 0; s2 < 4; ++s2) {
      const u32x4 v = *(const LAS u32x4*)(scr + s2 * 1024 + lane * 16);
      const int vo = ((s2 == 3 && lane >= 32) ? 0x7f000000 : lane * 16) + so + s2 * 1024;
      __builtin_amdgcn_raw_buffer_store_b128(v, rw, vo, 0, 16);
    }
  };
#define SELF_PF(g) if (SELFPF) __builtin_amdgcn_raw_ptr_buffer_load_lds(rw, (LAS void*)(smem + DUMMY_OFF), 4, lane < 28 ? lane * 128 : 0x7ffffff0, nt * J_B + ((g) * 16 + mt) * KB_B, 0, 16)
  u32x2 E, tag;
  unsigned cbv;

  if (w >= 6) {
    const int pw = w - 6;
    __builtin_amdgcn_s_setprio(3);
    LAS char* scr = smem + SCR_OFF + (4 + pw) * SCR_W;
    u32x2 sA, sB;
    E_LOAD();
    { const u16* pc = cb + (lane & 15); asm volatile("global_load_ushort %0, %1, off" : "=v"(cbv) : "v"(pc) : "memory"); }
    SCALE_LOAD(sA, 4 + pw);
    SCALE_LOAD(sB, 6 + pw);
    i32x4 qa[8], qb[8];
    wait_until(T0, pw ? T_G5 : T_G4);
    RAW_LOAD(qa, 4 + pw);
    wait_until(T0, pw ? T_G7 : T_G6);
    RAW_LOAD(qb, 6 + pw);
    WAIT_V(16);
    asm volatile("" : "+v"(E), "+v"(cbv), "+v"(sA), "+v"(sB));
    tag.x = E.x + 1u;
    tag.y = E.y ^ 0x5bd1e995u;
    if (lane < 16) *(LAS float*)(scr + 3584 + lane * 4) = __uint_as_float(cbv << 16);
    WAIT_V(8);
    STAMP(1);
    dequant2(qa, scr, sA, 0);
    dequant2(qa, scr, sA, 2);
    publish(scr, 4 + pw);
    STAMP(2);
    WAIT_V(4);
    STAMP(4);
    dequant2(qb, scr, sB, 0);
    dequant2(qb, scr, sB, 2);
    WAIT_V(0);
    FLAG_STORE(4 + pw);
    SELF_PF(4 + pw);
    publish(scr, 6 + pw);
    STAMP(3);
#define PBAR(t) do { STAMP(9 + (t)); BAR(); if ((t) == 4) CSTAMP(28); if ((t) == 14) CSTAMP(29); } while (0)
    BAR();
    PBAR(0);
    WAIT_V(0);
    FLAG_STORE(6 + pw);
    SELF_PF(6 + pw);
    PBAR(1); PBAR(2); PBAR(3); PBAR(4); PBAR(5); PBAR(6); PBAR(7);
    PBAR(8); PBAR(9); PBAR(10); PBAR(11); PBAR(12); PBAR(13); PBAR(14); PBAR(15);
  } else if (w >= 4) {
    const int lw = w - 4;
    const __amdgpu_buffer_rsrc_t rx = __builtin_amdgcn_make_buffer_rsrc((void*)x, 0, M_ * K_ * 2, 0x00020000);
    const int p = lane & 7, rl = lane >> 3, h = lane >> 4;
    int vx[2];
#pragma unroll
    for (int par = 0; par < 2; ++par) vx[par] = rl * (K_ * 2) + (p ^ (4 * par + h)) * 16;
    const int vw = lane * 16;
    const int sx0 = (m0 + lw * 64) * (K_ * 2);
    const int sw0 = nt * J_B + lw * 14 * 1024;
    auto issue_xwarm = [&](int piece) {
      __builtin_amdgcn_raw_ptr_buffer_load_lds(rx, (LAS void*)(smem + DUMMY_OFF), 4,
                                               (lane < 32 && piece >= 0) ? (lane >> 2) * (K_ * 2) + (lane & 3) * 128 : 0x7ffffff0,
                                               b * 8 * (K_ * 2) + (piece < 0 ? 0 : piece) * 512, 0, 0);
    };
    auto issue_x = [&](int kt, int slot) {
      LAS char* base = smem + slot * SLOT_B;
#pragma unroll
      for (int i = 0; i < 8; ++i)
        __builtin_amdgcn_raw_ptr_buffer_load_lds(rx, (LAS void*)(base + (lw * 8 + i) * 1024), 16, vx[i & 1], sx0 + i * 8 * (K_ * 2) + kt * 128, 0, 0);
    };
    auto issue_w = [&](int kt, int slot) {
      LAS char* base = smem + slot * SLOT_B;
#pragma unroll
      for (int i = 0; i < 14; ++i)
        __builtin_amdgcn_raw_ptr_buffer_load_lds(rw, (LAS void*)(base + XT_B + (lw * 14 + i) * 1024), 16, vw, sw0 + i * 1024 + kt * (8 * KB_B), 0, 16);
    };
    const char* pf = ws + FLAG_OFF + nt * 8 * 128 + (lane & 15) * 8;
    u32x2 Fa, Fb, Fs;
#define POLL_ISSUE(F, g) do { const char* pp_ = pf + (g) * 128; asm volatile("global_load_dwordx2 %0, %1, off sc1" : "=v"(F) : "v"(pp_) : "memory"); } while (0)
#define POLL_OK(F) (__builtin_amdgcn_ballot_w64(F.x == tag.x && F.y == tag.y) == ~0ull)
#define PIN(F) asm volatile("" : "+v"(F))
    if (lw == 0) { *(volatile LAS unsigned*)(smem + CNT_OFF) = 0u; WAIT_L0(); }
    E_LOAD();
    wait_until(T0, T_XWA);
    issue_xwarm(lw == 0 ? 0 : -1);
    WAIT_V(1);
    asm volatile("" : "+v"(E));
    tag.x = E.x + 1u;
    tag.y = E.y ^ 0x5bd1e995u;
    wait_until(T0, T_POLL);
    STAMP(1);
    auto spin = [&](int g) {
      const unsigned long long t0 = __builtin_amdgcn_s_memrealtime();
      for (;;) {
        POLL_ISSUE(Fs, g);
        WAIT_V(0);
        PIN(Fs);
        if (POLL_OK(Fs)) break;
        if (__builtin_amdgcn_s_memrealtime() - t0 > 2000000ull) break;
        __builtin_amdgcn_s_sleep(1);
      }
    };
    spin(0);
    STAMP(2);
    issue_x(0, 0);
    issue_w(0, 0);
    POLL_ISSUE(Fa, 1);
    POLL_ISSUE(Fb, 2);
    issue_x(1, 1);
    issue_w(1, 1);
    if (XWPOS == 0) { issue_xwarm(lw == 0 ? 1 : -1); issue_xwarm(2 + lw); }
    if (XWPOS == 0) WAIT_V(26); else WAIT_V(24);
    STAMP(3);
    BAR();
    if (XWPOS == 0) WAIT_V(24); else WAIT_V(22);
    PIN(Fa); PIN(Fb);
    if (!POLL_OK(Fa)) spin(1);
    int slot = 2;
#define TRIP(j, FNEW, FOLD, W1, XW, W2) do { \
      issue_x(2 * (j) + 2, slot); \
      issue_w(2 * (j) + 2, slot); \
      slot = slot == 2 ? 0 : slot + 1; \
      if (XW) { issue_xwarm(lw == 0 ? 1 : -1); issue_xwarm(2 + lw); } \
      WAIT_V(W1); \
      STAMP(9 + 2 * (j)); \
      BAR(); \
      issue_x(2 * (j) + 3, slot); \
      issue_w(2 * (j) + 3, slot); \
      slot = slot == 2 ? 0 : slot + 1; \
      POLL_ISSUE(FNEW, (j) + 3 < 8 ? (j) + 3 : 7); \
      WAIT_V(W2); \
      PIN(FOLD); \
      const bool ok_ = POLL_OK(FOLD); \
      STAMP(10 + 2 * (j)); \
      BAR(); \
      if (!ok_ && (j) + 2 < 8) spin((j) + 2); \
    } while (0)
    if (XWPOS == 0) TRIP(0, Fa, Fb, 24, 0, 23); else TRIP(0, Fa, Fb, 24, 1, 25);
#pragma unroll 1
    for (int j = 1; j < 7; j += 2) {
      TRIP(j, Fb, Fa, 23, 0, 23);
      if (j == 5 && mt == 0 && lw == 0 && lane == 0) {
        const u32x2 ne = {E.x + 1u, E.y};
        asm volatile("global_store_dwordx2 %0, %1, off sc1" ::"v"(pe), "v"(ne) : "memory");
      }
      TRIP(j + 1, Fa, Fb, 23, 0, 23);
    }
    WAIT_V(0);
    BAR();
    BAR();
  } else {
    const int wm = w & 1, wn = w >> 1;
    {
      LAS char* scr = smem + SCR_OFF + w * SCR_W;
      u32x2 sA;
      i32x4 q[8];
      E_LOAD();
      { const u16* pc = cb + (lane & 15); asm volatile("global_load_ushort %0, %1, off" : "=v"(cbv) : "v"(pc) : "memory"); }
      SCALE_LOAD(sA, w);
      if (w) wait_until(T0, w == 1 ? T_G1 : w == 2 ? T_G2 : T_G3);
      RAW_LOAD(q, w);
      WAIT_V(8);
      asm volatile("" : "+v"(E), "+v"(cbv), "+v"(sA));
      tag.x = E.x + 1u;
      tag.y = E.y ^ 0x5bd1e995u;
      if (lane < 16) *(LAS float*)(scr + 3584 + lane * 4) = __uint_as_float(cbv << 16);
      WAIT_V(0);
      STAMP(1);
      dequant2(q, scr, sA, 0);
      dequant2(q, scr, sA, 2);
      publish(scr, w);
      STAMP(2);
      WAIT_V(0);
      FLAG_STORE(w);
      SELF_PF(w);
      STAMP(3);
    }
    const int fr = lane & 15, fh = lane >> 4;
    const int xa0 = (wm * 64 + fr) * 128 + (fh ^ (fr >> 1)) * 16;
    const int wa0 = XT_B + fh * KB_B + (wn * 112 + fr) * 16;
    f32x4 acc[4][7];
#pragma unroll
    for (int mi = 0; mi < 4; ++mi)
#pragma unroll
      for (int ni = 0; ni < 7; ++ni) acc[mi][ni] = (f32x4){0.f, 0.f, 0.f, 0.f};
    bf16x8 X0[4], W0[7], X1[4], W1[7];
#define RD(XF, WF, so, ks) do { \
    _Pragma("unroll") for (int ni = 0; ni < 7; ++ni) WF[ni] = *(const LAS bf16x8*)(smem + (so) + wa0 + (ks) * 4 * KB_B + ni * 256); \
    _Pragma("unroll") for (int mi = 0; mi < 4; ++mi) XF[mi] = *(const LAS bf16x8*)(smem + (so) + ((xa0 ^ ((ks) * 64))) + mi * 2048); } while (0)
#define MM(XF, WF) do { \
    _Pragma("unroll") for (int mi = 0; mi < 4; ++mi) _Pragma("unroll") for (int ni = 0; ni < 7; ++ni) \
      acc[mi][ni] = __builtin_amdgcn_mfma_f32_16x16x32_bf16(WF[ni], XF[mi], acc[mi][ni], 0, 0, 0); } while (0)
#define ILV() do { _Pragma("unroll") for (int g_ = 0; g_ < 11; ++g_) { \
      __builtin_amdgcn_sched_group_barrier(0x100, 1, 0); __builtin_amdgcn_sched_group_barrier(0x8, 2, 0); } \
      __builtin_amdgcn_sched_group_barrier(0x8, 6, 0); } while (0)
    STAMP(4);
    BAR();
    STAMP(8);
    CSTAMP(26);
    int so = 0;
    RD(X0, W0, so, 0);
#pragma unroll 1
    for (int t = 0; t < 15; ++t) {
      WAIT_L0();
      __builtin_amdgcn_sched_barrier(0);
      RD(X1, W1, so, 1);
      MM(X0, W0);
      ILV();
      __builtin_amdgcn_sched_barrier(0);
      WAIT_L0();
      STAMP(9 + t);
      BAR();
      __builtin_amdgcn_sched_barrier(0);
      so = so == 2 * SLOT_B ? 0 : so + SLOT_B;
      RD(X0, W0, so, 0);
      MM(X1, W1);
      ILV();
      __builtin_amdgcn_sched_barrier(0);
    }
    WAIT_L0();
    __builtin_amdgcn_sched_barrier(0);
#define MMROW(XF, WF, mi) do { _Pragma("unroll") for (int ni = 0; ni < 7; ++ni) \
      acc[mi][ni] = __builtin_amdgcn_mfma_f32_16x16x32_bf16(WF[ni], XF[mi], acc[mi][ni], 0, 0, 0); } while (0)
#define CVTROW(mi) do { _Pragma("unroll") for (int ni = 0; ni < 7; ++ni) { \
        const u32x2 o_ = {pk_bf16(acc[mi][ni][0], acc[mi][ni][1]), pk_bf16(acc[mi][ni][2], acc[mi][ni][3])}; \
        *(LAS u32x2*)(smem + (wm * 64 + (mi) * 16 + fr) * OPITCH + (wn * 112 + ni * 16 + fh * 4) * 2) = o_; } \
      __builtin_amdgcn_sched_barrier(0); \
      WAIT_L0(); \
      if (lane == 0) __hip_atomic_fetch_add((LAS unsigned*)(smem + CNT_OFF), 1u << (8 * (mi)), __ATOMIC_RELAXED, __HIP_MEMORY_SCOPE_WORKGROUP); } while (0)
    RD(X1, W1, so, 1);
    __builtin_amdgcn_sched_barrier(0);
    MMROW(X0, W0, 0);
    __builtin_amdgcn_sched_barrier(0);
    WAIT_L0();
    STAMP(24);
    BAR();
    MMROW(X1, W1, 0);
    MMROW(X0, W0, 1);
    CVTROW(0);
    MMROW(X1, W1, 1);
    MMROW(X0, W0, 2);
    CVTROW(1);
    MMROW(X1, W1, 2);
    MMROW(X0, W0, 3);
    CVTROW(2);
    MMROW(X1, W1, 3);
    STAMP(5);
    CSTAMP(27);
    CVTROW(3);
    STAMP(6);
  }
  if (w >= 4) {
    const __amdgpu_buffer_rsrc_t ro = __builtin_amdgcn_make_buffer_rsrc((void*)out, 0, M_ * N_ * 2, 0x00020000);
    const int hh = w - 4;
    int lo_[4], go_[4];
#pragma unroll
    for (int i = 0; i < 4; ++i) {
      const int k = hh + 4 * i, c = k * 64 + lane, row32 = c / 28, ch = c - row32 * 28;
      const int row = (row32 >> 4) * 64 + (row32 & 15);
      lo_[i] = k < 14 ? row * OPITCH + ch * 16 : 0;
      go_[i] = k < 14 ? (m0 + row) * (N_ * 2) + n0 * 2 + ch * 16 : 0x7f000000;
    }
#pragma unroll
    for (int r = 0; r < 4; ++r) {
      while (((*(volatile LAS unsigned*)(smem + CNT_OFF) >> (8 * r)) & 0xffu) < 4u) __builtin_amdgcn_s_sleep(0);
      asm volatile("" ::: "memory");
      u32x4 v[4];
#pragma unroll
      for (int i = 0; i < 4; ++i) v[i] = *(const LAS u32x4*)(smem + lo_[i] + r * 16 * OPITCH);
#pragma unroll
      for (int i = 0; i < 4; ++i) __builtin_amdgcn_raw_buffer_store_b128(v[i], ro, go_[i] + (go_[i] < 0x7f000000 ? r * 16 * (N_ * 2) : 0), 0, OAUX);
    }
    STAMP(6);
  }
}

extern "C" void kernel_launch(void* const* d_in, const int* in_sizes, int n_in, void* d_out, int out_size, void* d_ws, size_t ws_size,
                              hipStream_t stream) {
  const u16* x = (const u16*)d_in[0];
  const int* qw = (const int*)d_in[1];
  const u16* cb = (const u16*)d_in[2];
  const u16* am = (const u16*)d_in[3];
  (void)hipFuncSetAttribute((const void*)k_fused, hipFuncAttributeMaxDynamicSharedMemorySize, LDS_B);
  k_fused<<<256, 512, LDS_B, stream>>>(x, qw, cb, am, (char*)d_ws, (u16*)d_out);
}
```

```cpp
#include <hip/hip_runtime.h>
#include <stdint.h>
typedef unsigned short u16;
typedef __attribute__((ext_vector_type(8))) __bf16 bf16x8;
typedef __attribute__((ext_vector_type(2))) __bf16 bf16x2;
typedef float f32x4 __attribute__((ext_vector_type(4)));
typedef float f32x2 __attribute__((ext_vector_type(2)));
typedef int i32x4 __attribute__((ext_vector_type(4)));
typedef unsigned u32x4 __attribute__((ext_vector_type(4)));
typedef unsigned u32x2 __attribute__((ext_vector_type(2)));
#define LAS __attribute__((address_space(3)))

constexpr int M_ = 2048, N_ = 3584, K_ = 1024;
constexpr int BM = 128, BN = 224;
constexpr int XT_B = BM * 64 * 2;
constexpr int WT_B = BN * 64 * 2;
constexpr int SLOT_B = XT_B + WT_B;
constexpr int NSLOT = 3;
constexpr int KB_B = BN * 16;
constexpr int J_B = (K_ / 8) * KB_B;
constexpr int W_TOTAL = (N_ / BN) * J_B;
constexpr int FLAG_OFF = W_TOTAL;
constexpr int EPOCH_OFF = FLAG_OFF + 128 * 128;
constexpr int SCR_OFF = NSLOT * SLOT_B;
constexpr int SCR_W = 3712;
constexpr int DUMMY_OFF = SCR_OFF + 6 * SCR_W;
constexpr int SIG_OFF = DUMMY_OFF + 256;
constexpr int CNT_OFF = SIG_OFF + 16;
constexpr int LDS_B = CNT_OFF + 16;
constexpr int OPITCH = 464;

#define T_G1 45
#define T_G2 70
#define T_G3 95
#define T_G4 60
#define T_G5 85
#define T_G6 0
#define T_G7 0
#define T_XWA 25
#define T_XWB 60
#define T_POLL 0
#define EPI 2
#define OAUX 2
#define XWPOS 1
#define TLBW 0
#define SELFPF 0

__device__ __forceinline__ unsigned pk_bf16(float a, float b) {
  f32x2 v = {a, b};
  bf16x2 r = __builtin_convertvector(v, bf16x2);
  return __builtin_bit_cast(unsigned, r);
}
__device__ __forceinline__ float bf_lo(unsigned u) { return __uint_as_float(u << 16); }
__device__ __forceinline__ float bf_hi(unsigned u) { return __uint_as_float(u & 0xffff0000u); }

#define WAIT_V(n) asm volatile("s_waitcnt vmcnt(%0)" ::"n"(n) : "memory")
#define WAIT_L0() __builtin_amdgcn_s_waitcnt(0xC07F)
#define BAR() do { asm volatile("" ::: "memory"); __builtin_amdgcn_s_barrier(); asm volatile("" ::: "memory"); } while (0)

#define STAMP(i)
#define CSTAMP(i)
#define DIAG_ARG

__device__ __forceinline__ void wait_until(unsigned long long t0, unsigned ticks) {
  while (__builtin_amdgcn_s_memrealtime() - t0 < ticks) __builtin_amdgcn_s_sleep(1);
}

__global__ __launch_bounds__(512, 2) void k_fused(const u16* __restrict__ x, const int* __restrict__ qw, const u16* __restrict__ cb,
                                                  const u16* __restrict__ am, char* __restrict__ ws, u16* __restrict__ out DIAG_ARG) {
  extern __shared__ __attribute__((aligned(16))) char smem_g[];
  LAS char* smem = (LAS char*)smem_g;
  const unsigned long long T0 = __builtin_amdgcn_s_memrealtime();
  const int tid = threadIdx.x, lane = tid & 63;
  const int w = __builtin_amdgcn_readfirstlane(tid >> 6);
  const int b = blockIdx.x, xcd = b & 7, bi = b >> 3;
#define MAP 0
  const int mt = MAP ? (bi & 15) : (xcd & 1) * 8 + (bi & 7), nt = MAP ? xcd * 2 + (bi >> 4) : (xcd >> 1) * 4 + (bi >> 3);
  const int m0 = mt * BM, n0 = nt * BN;
  STAMP(0);
  const __amdgpu_buffer_rsrc_t rw = __builtin_amdgcn_make_buffer_rsrc((void*)ws, 0, EPOCH_OFF + 16 * 128, 0x00020000);
  const char* pe = ws + EPOCH_OFF + nt * 128;
  char* pfl = ws + FLAG_OFF + nt * 8 * 128 + mt * 8;

  const int cbk = lane < 56 ? lane : 55;
  const int* qsrc = qw + (size_t)(mt * 8) * N_ + n0 + cbk * 4;
#define RAW_LOAD(q, g) do { const int* s_ = qsrc + (size_t)(g) * 128 * N_; _Pragma("unroll") for (int r = 0; r < 8; ++r) { const int* p_ = s_ + (size_t)r * N_; \
      asm volatile("global_load_dwordx4 %0, %1, off nt" : "=v"(q[r]) : "v"(p_) : "memory"); } } while (0)
#define SCALE_LOAD(dst, g) do { const u16* p_ = am + (size_t)(g) * N_ + n0 + cbk * 4; asm volatile("global_load_dwordx2 %0, %1, off" : "=v"(dst) : "v"(p_) : "memory"); } while (0)
#define E_LOAD() asm volatile("global_load_dwordx2 %0, %1, off sc1" : "=v"(E) : "v"(pe) : "memory")
#define FLAG_STORE(g) do { char* p_ = pfl + (g) * 128; asm volatile("global_store_dwordx2 %0, %1, off sc1" ::"v"(p_), "v"(tag) : "memory"); } while (0)
  auto dequant2 = [&](i32x4 (&q)[8], LAS char* scr, const u32x2 sc, int c0) {
#pragma unroll
    for (int r = 0; r < 8; ++r) asm volatile("" : "+v"(q[r]));
    const LAS float* lut = (const LAS float*)(scr + 3584);
    float f[2][8];
#pragma unroll
    for (int c = 0; c < 2; ++c)
#pragma unroll
      for (int r = 0; r < 8; ++r) f[c][r] = lut[q[r][c0 + c]];
    __builtin_amdgcn_sched_group_barrier(0x2, 16, 0);
    __builtin_amdgcn_sched_group_barrier(0x100, 16, 0);
    const float s0 = c0 ? bf_lo(sc.y) : bf_lo(sc.x), s1 = c0 ? bf_hi(sc.y) : bf_hi(sc.x);
#pragma unroll
    for (int c = 0; c < 2; ++c) {
      const float sv = c ? s1 : s0;
      u32x4 o;
#pragma unroll
      for (int pp = 0; pp < 4; ++pp) o[pp] = pk_bf16(f[c][2 * pp] * sv, f[c][2 * pp + 1] * sv);
      if (lane < 56) *(LAS u32x4*)(scr + lane * 64 + (c0 + c) * 16) = o;
    }
  };
  auto publish = [&](LAS char* scr, int g) {
    const int so = nt * J_B + (g * 16 + mt) * KB_B;
#pragma unroll
    for (int s2 = 0; s2 < 4; ++s2) {
      const u32x4 v = *(const LAS u32x4*)(scr + s2 * 1024 + lane * 16);
      const int vo = ((s2 == 3 && lane >= 32) ? 0x7f000000 : lane * 16) + so + s2 * 1024;
      __builtin_amdgcn_raw_buffer_store_b128(v, rw, vo, 0, 16);
    }
  };
#define SELF_PF(g) if (SELFPF) __builtin_amdgcn_raw_ptr_buffer_load_lds(rw, (LAS void*)(smem + DUMMY_OFF), 4, lane < 28 ? lane * 128 : 0x7ffffff0, nt * J_B + ((g) * 16 + mt) * KB_B, 0, 16)
  u32x2 E, tag;
  unsigned cbv;

  if (w >= 6) {
    const int pw = w - 6;
    __builtin_amdgcn_s_setprio(3);
    LAS char* scr = smem + SCR_OFF + (4 + pw) * SCR_W;
    u32x2 sA, sB;
    E_LOAD();
    { const u16* pc = cb + (lane & 15); asm volatile("global_load_ushort %0, %1, off" : "=v"(cbv) : "v"(pc) : "memory"); }
    SCALE_LOAD(sA, 4 + pw);
    SCALE_LOAD(sB, 6 + pw);
    i32x4 qa[8], qb[8];
    wait_until(T0, pw ? T_G5 : T_G4);
    RAW_LOAD(qa, 4 + pw);
    WAIT_V(8);
    asm volatile("" : "+v"(E), "+v"(cbv), "+v"(sA), "+v"(sB));
    tag.x = E.x + 1u;
    tag.y = E.y ^ 0x5bd1e995u;
    if (lane < 16) *(LAS float*)(scr + 3584 + lane * 4) = __uint_as_float(cbv << 16);
    WAIT_V(0);
    wait_until(T0, pw ? T_G7 : T_G6);
    RAW_LOAD(qb, 6 + pw);
    STAMP(1);
    dequant2(qa, scr, sA, 0);
    dequant2(qa, scr, sA, 2);
    publish(scr, 4 + pw);
    STAMP(2);
    WAIT_V(4);
    STAMP(4);
    dequant2(qb, scr, sB, 0);
    dequant2(qb, scr, sB, 2);
    WAIT_V(0);
    FLAG_STORE(4 + pw);
    SELF_PF(4 + pw);
    publish(scr, 6 + pw);
    STAMP(3);
#define PBAR(t) do { STAMP(9 + (t)); BAR(); if ((t) == 4) CSTAMP(28); if ((t) == 14) CSTAMP(29); } while (0)
    BAR();
    PBAR(0);
    WAIT_V(0);
    FLAG_STORE(6 + pw);
    SELF_PF(6 + pw);
    PBAR(1); PBAR(2); PBAR(3); PBAR(4); PBAR(5); PBAR(6); PBAR(7);
    PBAR(8); PBAR(9); PBAR(10); PBAR(11); PBAR(12); PBAR(13); PBAR(14); PBAR(15);
  } else if (w >= 4) {
    const int lw = w - 4;
    const __amdgpu_buffer_rsrc_t rx = __builtin_amdgcn_make_buffer_rsrc((void*)x, 0, M_ * K_ * 2, 0x00020000);
    const int p = lane & 7, rl = lane >> 3, h = lane >> 4;
    int vx[2];
#pragma unroll
    for (int par = 0; par < 2; ++par) vx[par] = rl * (K_ * 2) + (p ^ (4 * par + h)) * 16;
    const int vw = lane * 16;
    const int sx0 = (m0 + lw * 64) * (K_ * 2);
    const int sw0 = nt * J_B + lw * 14 * 1024;
    auto issue_xwarm = [&](int piece) {
      __builtin_amdgcn_raw_ptr_buffer_load_lds(rx, (LAS void*)(smem + DUMMY_OFF), 4,
                                               (lane < 32 && piece >= 0) ? (lane >> 2) * (K_ * 2) + (lane & 3) * 128 : 0x7ffffff0,
                                               b * 8 * (K_ * 2) + (piece < 0 ? 0 : piece) * 512, 0, 0);
    };
    auto issue_x = [&](int kt, int slot) {
      LAS char* base = smem + slot * SLOT_B;
#pragma unroll
      for (int i = 0; i < 8; ++i)
        __builtin_amdgcn_raw_ptr_buffer_load_lds(rx, (LAS void*)(base + (lw * 8 + i) * 1024), 16, vx[i & 1], sx0 + i * 8 * (K_ * 2) + kt * 128, 0, 0);
    };
    auto issue_w = [&](int kt, int slot) {
      LAS char* base = smem + slot * SLOT_B;
#pragma unroll
      for (int i = 0; i < 14; ++i)
        __builtin_amdgcn_raw_ptr_buffer_load_lds(rw, (LAS void*)(base + XT_B + (lw * 14 + i) * 1024), 16, vw, sw0 + i * 1024 + kt * (8 * KB_B), 0, 16);
    };
    const char* pf = ws + FLAG_OFF + nt * 8 * 128 + (lane & 15) * 8;
    u32x2 Fa, Fb, Fs;
#define POLL_ISSUE(F, g) do { const char* pp_ = pf + (g) * 128; asm volatile("global_load_dwordx2 %0, %1, off sc1" : "=v"(F) : "v"(pp_) : "memory"); } while (0)
#define POLL_OK(F) (__builtin_amdgcn_ballot_w64(F.x == tag.x && F.y == tag.y) == ~0ull)
#define PIN(F) asm volatile("" : "+v"(F))
    if (lw == 0) { *(volatile LAS unsigned*)(smem + CNT_OFF) = 0u; *(volatile LAS unsigned*)(smem + SIG_OFF) = 0u; WAIT_L0(); }
    E_LOAD();
    wait_until(T0, T_XWA);
    issue_xwarm(lw == 0 ? 0 : -1);
    WAIT_V(1);
    asm volatile("" : "+v"(E));
    tag.x = E.x + 1u;
    tag.y = E.y ^ 0x5bd1e995u;
    {
      const unsigned long long t0 = __builtin_amdgcn_s_memrealtime();
      while (*(volatile LAS unsigned*)(smem + SIG_OFF) != 0x600dbeefu && __builtin_amdgcn_s_memrealtime() - t0 < 2000000ull) __builtin_amdgcn_s_sleep(1);
    }
    wait_until(T0, T_POLL);
    STAMP(1);
    auto spin = [&](int g) {
      const unsigned long long t0 = __builtin_amdgcn_s_memrealtime();
      for (;;) {
        POLL_ISSUE(Fs, g);
        WAIT_V(0);
        PIN(Fs);
        if (POLL_OK(Fs)) break;
        if (__builtin_amdgcn_s_memrealtime() - t0 > 2000000ull) break;
        __builtin_amdgcn_s_sleep(1);
      }
    };
    spin(0);
    STAMP(2);
    issue_x(0, 0);
    issue_w(0, 0);
    POLL_ISSUE(Fa, 1);
    POLL_ISSUE(Fb, 2);
    issue_x(1, 1);
    issue_w(1, 1);
    if (XWPOS == 0) { issue_xwarm(lw == 0 ? 1 : -1); issue_xwarm(2 + lw); }
    if (XWPOS == 0) WAIT_V(26); else WAIT_V(24);
    STAMP(3);
    BAR();
    if (XWPOS == 0) WAIT_V(24); else WAIT_V(22);
    PIN(Fa); PIN(Fb);
    if (!POLL_OK(Fa)) spin(1);
    int slot = 2;
#define TRIP(j, FNEW, FOLD, W1, XW, W2) do { \
      issue_x(2 * (j) + 2, slot); \
      issue_w(2 * (j) + 2, slot); \
      slot = slot == 2 ? 0 : slot + 1; \
      if (XW) { issue_xwarm(lw == 0 ? 1 : -1); issue_xwarm(2 + lw); } \
      WAIT_V(W1); \
      STAMP(9 + 2 * (j)); \
      BAR(); \
      issue_x(2 * (j) + 3, slot); \
      issue_w(2 * (j) + 3, slot); \
      slot = slot == 2 ? 0 : slot + 1; \
      POLL_ISSUE(FNEW, (j) + 3 < 8 ? (j) + 3 : 7); \
      WAIT_V(W2); \
      PIN(FOLD); \
      const bool ok_ = POLL_OK(FOLD); \
      STAMP(10 + 2 * (j)); \
      BAR(); \
      if (!ok_ && (j) + 2 < 8) spin((j) + 2); \
    } while (0)
    if (XWPOS == 0) TRIP(0, Fa, Fb, 24, 0, 23); else TRIP(0, Fa, Fb, 24, 1, 25);
#pragma unroll 1
    for (int j = 1; j < 7; j += 2) {
      TRIP(j, Fb, Fa, 23, 0, 23);
      if (j == 5 && mt == 0 && lw == 0 && lane == 0) {
        const u32x2 ne = {E.x + 1u, E.y};
        asm volatile("global_store_dwordx2 %0, %1, off sc1" ::"v"(pe), "v"(ne) : "memory");
      }
      TRIP(j + 1, Fa, Fb, 23, 0, 23);
    }
    WAIT_V(0);
    BAR();
    BAR();
  } else {
    const int wm = w & 1, wn = w >> 1;
    {
      LAS char* scr = smem + SCR_OFF + w * SCR_W;
      u32x2 sA;
      i32x4 q[8];
      E_LOAD();
      { const u16* pc = cb + (lane & 15); asm volatile("global_load_ushort %0, %1, off" : "=v"(cbv) : "v"(pc) : "memory"); }
      SCALE_LOAD(sA, w);
      if (w) wait_until(T0, w == 1 ? T_G1 : w == 2 ? T_G2 : T_G3);
      RAW_LOAD(q, w);
      WAIT_V(8);
      asm volatile("" : "+v"(E), "+v"(cbv), "+v"(sA));
      tag.x = E.x + 1u;
      tag.y = E.y ^ 0x5bd1e995u;
      if (lane < 16) *(LAS float*)(scr + 3584 + lane * 4) = __uint_as_float(cbv << 16);
      WAIT_V(0);
      STAMP(1);
      dequant2(q, scr, sA, 0);
      dequant2(q, scr, sA, 2);
      publish(scr, w);
      STAMP(2);
      WAIT_V(0);
      FLAG_STORE(w);
      if (w == 0) *(volatile LAS unsigned*)(smem + SIG_OFF) = 0x600dbeefu;
      SELF_PF(w);
      STAMP(3);
    }
    const int fr = lane & 15, fh = lane >> 4;
    const int xa0 = (wm * 64 + fr) * 128 + (fh ^ (fr >> 1)) * 16;
    const int wa0 = XT_B + fh * KB_B + (wn * 112 + fr) * 16;
    f32x4 acc[4][7];
#pragma unroll
    for (int mi = 0; mi < 4; ++mi)
#pragma unroll
      for (int ni = 0; ni < 7; ++ni) acc[mi][ni] = (f32x4){0.f, 0.f, 0.f, 0.f};
    bf16x8 X0[4], W0[7], X1[4], W1[7];
#define RD(XF, WF, so, ks) do { \
    _Pragma("unroll") for (int ni = 0; ni < 7; ++ni) WF[ni] = *(const LAS bf16x8*)(smem + (so) + wa0 + (ks) * 4 * KB_B + ni * 256); \
    _Pragma("unroll") for (int mi = 0; mi < 4; ++mi) XF[mi] = *(const LAS bf16x8*)(smem + (so) + ((xa0 ^ ((ks) * 64))) + mi * 2048); } while (0)
#define MM(XF, WF) do { \
    _Pragma("unroll") for (int mi = 0; mi < 4; ++mi) _Pragma("unroll") for (int ni = 0; ni < 7; ++ni) \
      acc[mi][ni] = __builtin_amdgcn_mfma_f32_16x16x32_bf16(WF[ni], XF[mi], acc[mi][ni], 0, 0, 0); } while (0)
#define ILV() do { _Pragma("unroll") for (int g_ = 0; g_ < 11; ++g_) { \
      __builtin_amdgcn_sched_group_barrier(0x100, 1, 0); __builtin_amdgcn_sched_group_barrier(0x8, 2, 0); } \
      __builtin_amdgcn_sched_group_barrier(0x8, 6, 0); } while (0)
    STAMP(4);
    BAR();
    STAMP(8);
    CSTAMP(26);
    int so = 0;
    RD(X0, W0, so, 0);
#pragma unroll 1
    for (int t = 0; t < 15; ++t) {
      WAIT_L0();
      __builtin_amdgcn_sched_barrier(0);
      RD(X1, W1, so, 1);
      MM(X0, W0);
      ILV();
      __builtin_amdgcn_sched_barrier(0);
      WAIT_L0();
      STAMP(9 + t);
      BAR();
      __builtin_amdgcn_sched_barrier(0);
      so = so == 2 * SLOT_B ? 0 : so + SLOT_B;
      RD(X0, W0, so, 0);
      MM(X1, W1);
      ILV();
      __builtin_amdgcn_sched_barrier(0);
    }
    WAIT_L0();
    __builtin_amdgcn_sched_barrier(0);
#define MMROW(XF, WF, mi) do { _Pragma("unroll") for (int ni = 0; ni < 7; ++ni) \
      acc[mi][ni] = __builtin_amdgcn_mfma_f32_16x16x32_bf16(WF[ni], XF[mi], acc[mi][ni], 0, 0, 0); } while (0)
#define CVTROW(mi) do { _Pragma("unroll") for (int ni = 0; ni < 7; ++ni) { \
        const u32x2 o_ = {pk_bf16(acc[mi][ni][0], acc[mi][ni][1]), pk_bf16(acc[mi][ni][2], acc[mi][ni][3])}; \
        *(LAS u32x2*)(smem + (wm * 64 + (mi) * 16 + fr) * OPITCH + (wn * 112 + ni * 16 + fh * 4) * 2) = o_; } \
      __builtin_amdgcn_sched_barrier(0); \
      WAIT_L0(); \
      if (lane == 0) __hip_atomic_fetch_add((LAS unsigned*)(smem + CNT_OFF), 1u << (8 * (mi)), __ATOMIC_RELAXED, __HIP_MEMORY_SCOPE_WORKGROUP); } while (0)
    RD(X1, W1, so, 1);
    __builtin_amdgcn_sched_barrier(0);
    MMROW(X0, W0, 0);
    __builtin_amdgcn_sched_barrier(0);
    WAIT_L0();
    STAMP(24);
    BAR();
    MMROW(X1, W1, 0);
    MMROW(X0, W0, 1);
    CVTROW(0);
    MMROW(X1, W1, 1);
    MMROW(X0, W0, 2);
    CVTROW(1);
    MMROW(X1, W1, 2);
    MMROW(X0, W0, 3);
    CVTROW(2);
    MMROW(X1, W1, 3);
    STAMP(5);
    CSTAMP(27);
    CVTROW(3);
    STAMP(6);
  }
  if (w >= 4) {
    const __amdgpu_buffer_rsrc_t ro = __builtin_amdgcn_make_buffer_rsrc((void*)out, 0, M_ * N_ * 2, 0x00020000);
    const int hh = w - 4;
    int lo_[4], go_[4];
#pragma unroll
    for (int i = 0; i < 4; ++i) {
      const int k = hh + 4 * i, c = k * 64 + lane, row32 = c / 28, ch = c - row32 * 28;
      const int row = (row32 >> 4) * 64 + (row32 & 15);
      lo_[i] = k < 14 ? row * OPITCH + ch * 16 : 0;
      go_[i] = k < 14 ? (m0 + row) * (N_ * 2) + n0 * 2 + ch * 16 : 0x7f000000;
    }
#pragma unroll
    for (int r = 0; r < 4; ++r) {
      while (((*(volatile LAS unsigned*)(smem + CNT_OFF) >> (8 * r)) & 0xffu) < 4u) __builtin_amdgcn_s_sleep(0);
      asm volatile("" ::: "memory");
      u32x4 v[4];
#pragma unroll
      for (int i = 0; i < 4; ++i) v[i] = *(const LAS u32x4*)(smem + lo_[i] + r * 16 * OPITCH);
#pragma unroll
      for (int i = 0; i < 4; ++i) __builtin_amdgcn_raw_buffer_store_b128(v[i], ro, go_[i] + (go_[i] < 0x7f000000 ? r * 16 * (N_ * 2) : 0), 0, OAUX);
    }
    STAMP(6);
  }
}

extern "C" void kernel_launch(void* const* d_in, const int* in_sizes, int n_in, void* d_out, int out_size, void* d_ws, size_t ws_size,
                              hipStream_t stream) {
  const u16* x = (const u16*)d_in[0];
  const int* qw = (const int*)d_in[1];
  const u16* cb = (const u16*)d_in[2];
  const u16* am = (const u16*)d_in[3];
  (void)hipFuncSetAttribute((const void*)k_fused, hipFuncAttributeMaxDynamicSharedMemorySize, LDS_B);
  k_fused<<<256, 512, LDS_B, stream>>>(x, qw, cb, am, (char*)d_ws, (u16*)d_out);
}
```

```cpp
#include <hip/hip_runtime.h>
#include <stdint.h>

typedef unsigned short u16;
typedef __attribute__((ext_vector_type(8))) __bf16 bf16x8;
typedef __attribute__((ext_vector_type(2))) __bf16 bf16x2;
typedef float f32x4 __attribute__((ext_vector_type(4)));
typedef float f32x2 __attribute__((ext_vector_type(2)));
typedef int i32x4 __attribute__((ext_vector_type(4)));
typedef unsigned u32x4 __attribute__((ext_vector_type(4)));
typedef unsigned u32x2 __attribute__((ext_vector_type(2)));
#define LAS __attribute__((address_space(3)))

constexpr int M_ = 2048, N_ = 3584, K_ = 1024;
constexpr int BM = 128, BN = 224;

constexpr int KB_B = BN * 16;
constexpr int J_B = (K_ / 8) * KB_B;
constexpr int FLAG_OFF = (N_ / BN) * J_B;
constexpr int EPOCH_OFF = FLAG_OFF + 128 * 128;
constexpr int WS_B = EPOCH_OFF + 16 * 128;

constexpr int XT_B = BM * 64 * 2;
constexpr int WT_B = BN * 64 * 2;
constexpr int SLOT_B = XT_B + WT_B;
constexpr int SCR_OFF = 3 * SLOT_B;
constexpr int SCR_W = 3712;
constexpr int DUMMY_OFF = SCR_OFF + 6 * SCR_W;
constexpr int SIG_OFF = DUMMY_OFF + 256;
constexpr int CNT_OFF = SIG_OFF + 16;
constexpr int LDS_B = CNT_OFF + 16;
constexpr int OPITCH = 464;

constexpr unsigned T_UNIT[6] = {0, 45, 70, 95, 60, 85};
constexpr unsigned T_XWARM = 25;
constexpr unsigned long long SPIN_LIMIT = 2000000ull;

__device__ __forceinline__ unsigned pk_bf16(float a, float b) {
  f32x2 v = {a, b};
  bf16x2 r = __builtin_convertvector(v, bf16x2);
  return __builtin_bit_cast(unsigned, r);
}
__device__ __forceinline__ float bf_lo(unsigned u) { return __uint_as_float(u << 16); }
__device__ __forceinline__ float bf_hi(unsigned u) { return __uint_as_float(u & 0xffff0000u); }
__device__ __forceinline__ void wait_until(unsigned long long t0, unsigned ticks) {
  while (__builtin_amdgcn_s_memrealtime() - t0 < ticks) __builtin_amdgcn_s_sleep(1);
}

#define WAIT_V(n) asm volatile("s_waitcnt vmcnt(%0)" ::"n"(n) : "memory")
#define WAIT_L0() __builtin_amdgcn_s_waitcnt(0xC07F)
#define BAR() do { asm volatile("" ::: "memory"); __builtin_amdgcn_s_barrier(); asm volatile("" ::: "memory"); } while (0)
#define PIN(v) asm volatile("" : "+v"(v))


__global__ __launch_bounds__(512, 2) void k_fused(const u16* __restrict__ x, const int* __restrict__ qw, const u16* __restrict__ cb,
                                                  const u16* __restrict__ am, char* __restrict__ ws, u16* __restrict__ out) {
  extern __shared__ __attribute__((aligned(16))) char smem_g[];
  LAS char* smem = (LAS char*)smem_g;
  const unsigned long long T0 = __builtin_amdgcn_s_memrealtime();
  const int tid = threadIdx.x, lane = tid & 63;
  const int w = __builtin_amdgcn_readfirstlane(tid >> 6);
  const int b = blockIdx.x, xcd = b & 7, bi = b >> 3;
  const int mt = (xcd & 1) * 8 + (bi & 7), nt = (xcd >> 1) * 4 + (bi >> 3);
  const int m0 = mt * BM, n0 = nt * BN;
  const __amdgpu_buffer_rsrc_t rw = __builtin_amdgcn_make_buffer_rsrc((void*)ws, 0, WS_B, 0x00020000);
  const char* pe = ws + EPOCH_OFF + nt * 128;
  char* pfl = ws + FLAG_OFF + nt * 8 * 128 + mt * 8;
  u32x2 E, tag;

  const int cbk = lane < 56 ? lane : 55;
  const int* qsrc = qw + (size_t)(mt * 8) * N_ + n0 + cbk * 4;
  unsigned cbv;
#define RAW_LOAD(q, g) do { const int* s_ = qsrc + (size_t)(g) * 128 * N_; _Pragma("unroll") for (int r = 0; r < 8; ++r) { const int* p_ = s_ + (size_t)r * N_; \
      asm volatile("global_load_dwordx4 %0, %1, off nt" : "=v"(q[r]) : "v"(p_) : "memory"); } } while (0)
#define SCALE_LOAD(dst, g) do { const u16* p_ = am + (size_t)(g) * N_ + n0 + cbk * 4; asm volatile("global_load_dwordx2 %0, %1, off" : "=v"(dst) : "v"(p_) : "memory"); } while (0)
#define CB_LOAD() do { const u16* p_ = cb + (lane & 15); asm volatile("global_load_ushort %0, %1, off" : "=v"(cbv) : "v"(p_) : "memory"); } while (0)
#define E_LOAD() asm volatile("global_load_dwordx2 %0, %1, off sc1" : "=v"(E) : "v"(pe) : "memory")
#define SET_TAG() do { PIN(E); tag.x = E.x + 1u; tag.y = E.y ^ 0x5bd1e995u; } while (0)
#define FLAG_STORE(g) do { char* p_ = pfl + (g) * 128; asm volatile("global_store_dwordx2 %0, %1, off sc1" ::"v"(p_), "v"(tag) : "memory"); } while (0)
  auto put_codebook = [&](LAS char* scr) {
    PIN(cbv);
    if (lane < 16) *(LAS float*)(scr + 3584 + lane * 4) = __uint_as_float(cbv << 16);
  };
  auto dequant2 = [&](i32x4 (&q)[8], LAS char* scr, const u32x2 sc, int c0) {
#pragma unroll
    for (int r = 0; r < 8; ++r) PIN(q[r]);
    const LAS float* lut = (const LAS float*)(scr + 3584);
    float f[2][8];
#pragma unroll
    for (int c = 0; c < 2; ++c)
#pragma unroll
      for (int r = 0; r < 8; ++r) f[c][r] = lut[q[r][c0 + c]];
    __builtin_amdgcn_sched_group_barrier(0x2, 16, 0);
    __builtin_amdgcn_sched_group_barrier(0x100, 16, 0);
    const float s0 = c0 ? bf_lo(sc.y) : bf_lo(sc.x), s1 = c0 ? bf_hi(sc.y) : bf_hi(sc.x);
#pragma unroll
    for (int c = 0; c < 2; ++c) {
      const float sv = c ? s1 : s0;
      u32x4 o;
#pragma unroll
      for (int pp = 0; pp < 4; ++pp) o[pp] = pk_bf16(f[c][2 * pp] * sv, f[c][2 * pp + 1] * sv);
      if (lane < 56) *(LAS u32x4*)(scr + lane * 64 + (c0 + c) * 16) = o;
    }
  };
  auto publish = [&](LAS char* scr, int g) {
    const int so = nt * J_B + (g * 16 + mt) * KB_B;
#pragma unroll
    for (int s2 = 0; s2 < 4; ++s2) {
      const u32x4 v = *(const LAS u32x4*)(scr + s2 * 1024 + lane * 16);
      const int vo = ((s2 == 3 && lane >= 32) ? 0x7f000000 : lane * 16) + so + s2 * 1024;
      __builtin_amdgcn_raw_buffer_store_b128(v, rw, vo, 0, 16);
    }
  };

  if (w >= 6) {
    const int pw = w - 6;
    __builtin_amdgcn_s_setprio(3);
    LAS char* scr = smem + SCR_OFF + (4 + pw) * SCR_W;
    u32x2 sA, sB;
    i32x4 qa[8], qb[8];
    E_LOAD();
    CB_LOAD();
    SCALE_LOAD(sA, 4 + pw);
    SCALE_LOAD(sB, 6 + pw);
    wait_until(T0, pw ? T_UNIT[5] : T_UNIT[4]);
    RAW_LOAD(qa, 4 + pw);
    WAIT_V(8);
    SET_TAG();
    PIN(sA); PIN(sB);
    put_codebook(scr);
    WAIT_V(0);
    RAW_LOAD(qb, 6 + pw);
    dequant2(qa, scr, sA, 0);
    dequant2(qa, scr, sA, 2);
    publish(scr, 4 + pw);
    WAIT_V(4);
    dequant2(qb, scr, sB, 0);
    dequant2(qb, scr, sB, 2);
    WAIT_V(0);
    FLAG_STORE(4 + pw);
    publish(scr, 6 + pw);
    BAR();
    BAR();
    WAIT_V(0);
    FLAG_STORE(6 + pw);
#pragma unroll
    for (int t = 2; t <= 16; ++t) BAR();
  } else if (w >= 4) {
    const int lw = w - 4;
    const __amdgpu_buffer_rsrc_t rx = __builtin_amdgcn_make_buffer_rsrc((void*)x, 0, M_ * K_ * 2, 0x00020000);
    const int p = lane & 7, rl = lane >> 3, h = lane >> 4;
    int vx[2];
#pragma unroll
    for (int par = 0; par < 2; ++par) vx[par] = rl * (K_ * 2) + (p ^ (4 * par + h)) * 16;
    const int vw = lane * 16;
    const int sx0 = (m0 + lw * 64) * (K_ * 2);
    const int sw0 = nt * J_B + lw * 14 * 1024;
    auto issue_x = [&](int kt, int slot) {
      LAS char* base = smem + slot * SLOT_B;
#pragma unroll
      for (int i = 0; i < 8; ++i)
        __builtin_amdgcn_raw_ptr_buffer_load_lds(rx, (LAS void*)(base + (lw * 8 + i) * 1024), 16, vx[i & 1], sx0 + i * 8 * (K_ * 2) + kt * 128, 0, 0);
    };
    auto issue_w = [&](int kt, int slot) {
      LAS char* base = smem + slot * SLOT_B;
#pragma unroll
      for (int i = 0; i < 14; ++i)
        __builtin_amdgcn_raw_ptr_buffer_load_lds(rw, (LAS void*)(base + XT_B + (lw * 14 + i) * 1024), 16, vw, sw0 + i * 1024 + kt * (8 * KB_B), 0, 16);
    };
    auto issue_xwarm = [&](int piece) {
      __builtin_amdgcn_raw_ptr_buffer_load_lds(rx, (LAS void*)(smem + DUMMY_OFF), 4,
                                               (lane < 32 && piece >= 0) ? (lane >> 2) * (K_ * 2) + (lane & 3) * 128 : 0x7ffffff0,
                                               b * 8 * (K_ * 2) + (piece < 0 ? 0 : piece) * 512, 0, 0);
    };
    const char* pf = ws + FLAG_OFF + nt * 8 * 128 + (lane & 15) * 8;
    u32x2 Fa, Fb, Fs;
#define POLL_ISSUE(F, g) do { const char* pp_ = pf + (g) * 128; asm volatile("global_load_dwordx2 %0, %1, off sc1" : "=v"(F) : "v"(pp_) : "memory"); } while (0)
#define POLL_OK(F) (__builtin_amdgcn_ballot_w64(F.x == tag.x && F.y == tag.y) == ~0ull)
    auto spin = [&](int g) {
      const unsigned long long t0 = __builtin_amdgcn_s_memrealtime();
      for (;;) {
        POLL_ISSUE(Fs, g);
        WAIT_V(0);
        PIN(Fs);
        if (POLL_OK(Fs)) break;
        if (__builtin_amdgcn_s_memrealtime() - t0 > SPIN_LIMIT) break;
        __builtin_amdgcn_s_sleep(1);
      }
    };
    if (lw == 0) {
      *(volatile LAS unsigned*)(smem + CNT_OFF) = 0u;
      *(volatile LAS unsigned*)(smem + SIG_OFF) = 0u;
      WAIT_L0();
    }
    E_LOAD();
    wait_until(T0, T_XWARM);
    issue_xwarm(lw == 0 ? 0 : -1);
    WAIT_V(1);
    SET_TAG();
    {
      const unsigned long long t0 = __builtin_amdgcn_s_memrealtime();
      while (*(volatile LAS unsigned*)(smem + SIG_OFF) != 0x600dbeefu && __builtin_amdgcn_s_memrealtime() - t0 < SPIN_LIMIT) __builtin_amdgcn_s_sleep(1);
    }
    spin(0);
    issue_x(0, 0);
    issue_w(0, 0);
    POLL_ISSUE(Fa, 1);
    POLL_ISSUE(Fb, 2);
    issue_x(1, 1);
    issue_w(1, 1);
    WAIT_V(24);
    BAR();
    WAIT_V(22);
    PIN(Fa); PIN(Fb);
    if (!POLL_OK(Fa)) spin(1);
    int slot = 2;
#define TRIP(j, FNEW, FOLD, W1, XW, W2) do { \
      issue_x(2 * (j) + 2, slot); \
      issue_w(2 * (j) + 2, slot); \
      slot = slot == 2 ? 0 : slot + 1; \
      if (XW) { issue_xwarm(lw == 0 ? 1 : -1); issue_xwarm(2 + lw); } \
      WAIT_V(W1); \
      BAR(); \
      issue_x(2 * (j) + 3, slot); \
      issue_w(2 * (j) + 3, slot); \
      slot = slot == 2 ? 0 : slot + 1; \
      POLL_ISSUE(FNEW, (j) + 3 < 8 ? (j) + 3 : 7); \
      WAIT_V(W2); \
      PIN(FOLD); \
      const bool ok_ = POLL_OK(FOLD); \
      BAR(); \
      if (!ok_ && (j) + 2 < 8) spin((j) + 2); \
    } while (0)
    TRIP(0, Fa, Fb, 24, 1, 25);
#pragma unroll 1
    for (int j = 1; j < 7; j += 2) {
      TRIP(j, Fb, Fa, 23, 0, 23);
      if (j == 5 && mt == 0 && lw == 0 && lane == 0) {
        const u32x2 ne = {E.x + 1u, E.y};
        asm volatile("global_store_dwordx2 %0, %1, off sc1" ::"v"(pe), "v"(ne) : "memory");
      }
      TRIP(j + 1, Fa, Fb, 23, 0, 23);
    }
    WAIT_V(0);
    BAR();
    BAR();
  } else {
    const int wm = w & 1, wn = w >> 1;
    {
      LAS char* scr = smem + SCR_OFF + w * SCR_W;
      u32x2 sA;
      i32x4 q[8];
      E_LOAD();
      CB_LOAD();
      SCALE_LOAD(sA, w);
      if (w) wait_until(T0, w == 1 ? T_UNIT[1] : w == 2 ? T_UNIT[2] : T_UNIT[3]);
      RAW_LOAD(q, w);
      WAIT_V(8);
      SET_TAG();
      PIN(sA);
      put_codebook(scr);
      WAIT_V(0);
      dequant2(q, scr, sA, 0);
      dequant2(q, scr, sA, 2);
      publish(scr, w);
      WAIT_V(0);
      FLAG_STORE(w);
      if (w == 0) *(volatile LAS unsigned*)(smem + SIG_OFF) = 0x600dbeefu;
    }
    const int fr = lane & 15, fh = lane >> 4;
    const int xa0 = (wm * 64 + fr) * 128 + (fh ^ (fr >> 1)) * 16;
    const int wa0 = XT_B + fh * KB_B + (wn * 112 + fr) * 16;
    f32x4 acc[4][7];
#pragma unroll
    for (int mi = 0; mi < 4; ++mi)
#pragma unroll
      for (int ni = 0; ni < 7; ++ni) acc[mi][ni] = (f32x4){0.f, 0.f, 0.f, 0.f};
    bf16x8 X0[4], W0[7], X1[4], W1[7];
#define RD(XF, WF, so, ks) do { \
    _Pragma("unroll") for (int ni = 0; ni < 7; ++ni) WF[ni] = *(const LAS bf16x8*)(smem + (so) + wa0 + (ks) * 4 * KB_B + ni * 256); \
    _Pragma("unroll") for (int mi = 0; mi < 4; ++mi) XF[mi] = *(const LAS bf16x8*)(smem + (so) + ((xa0 ^ ((ks) * 64))) + mi * 2048); } while (0)
#define MMROW(XF, WF, mi) do { _Pragma("unroll") for (int ni = 0; ni < 7; ++ni) \
      acc[mi][ni] = __builtin_amdgcn_mfma_f32_16x16x32_bf16(WF[ni], XF[mi], acc[mi][ni], 0, 0, 0); } while (0)
#define MM(XF, WF) do { _Pragma("unroll") for (int mi = 0; mi < 4; ++mi) MMROW(XF, WF, mi); } while (0)
#define ILV() do { _Pragma("unroll") for (int g_ = 0; g_ < 11; ++g_) {   \
      __builtin_amdgcn_sched_group_barrier(0x100, 1, 0); __builtin_amdgcn_sched_group_barrier(0x8, 2, 0); } \
      __builtin_amdgcn_sched_group_barrier(0x8, 6, 0); } while (0)
    BAR();
    int so = 0;
    RD(X0, W0, so, 0);
#pragma unroll 1
    for (int t = 0; t < 15; ++t) {
      WAIT_L0();
      __builtin_amdgcn_sched_barrier(0);
      RD(X1, W1, so, 1);
      MM(X0, W0);
      ILV();
      __builtin_amdgcn_sched_barrier(0);
      WAIT_L0();
      BAR();
      __builtin_amdgcn_sched_barrier(0);
      so = so == 2 * SLOT_B ? 0 : so + SLOT_B;
      RD(X0, W0, so, 0);
      MM(X1, W1);
      ILV();
      __builtin_amdgcn_sched_barrier(0);
    }
    WAIT_L0();
    __builtin_amdgcn_sched_barrier(0);
#define CVTROW(mi) do { _Pragma("unroll") for (int ni = 0; ni < 7; ++ni) { \
        const u32x2 o_ = {pk_bf16(acc[mi][ni][0], acc[mi][ni][1]), pk_bf16(acc[mi][ni][2], acc[mi][ni][3])}; \
        *(LAS u32x2*)(smem + (wm * 64 + (mi) * 16 + fr) * OPITCH + (wn * 112 + ni * 16 + fh * 4) * 2) = o_; } \
      __builtin_amdgcn_sched_barrier(0); \
      WAIT_L0(); \
      if (lane == 0) __hip_atomic_fetch_add((LAS unsigned*)(smem + CNT_OFF), 1u << (8 * (mi)), __ATOMIC_RELAXED, __HIP_MEMORY_SCOPE_WORKGROUP); } while (0)
    RD(X1, W1, so, 1);
    __builtin_amdgcn_sched_barrier(0);
    MMROW(X0, W0, 0);
    __builtin_amdgcn_sched_barrier(0);
    WAIT_L0();
    BAR();
    MMROW(X1, W1, 0);
    MMROW(X0, W0, 1);
    CVTROW(0);
    MMROW(X1, W1, 1);
    MMROW(X0, W0, 2);
    CVTROW(1);
    MMROW(X1, W1, 2);
    MMROW(X0, W0, 3);
    CVTROW(2);
    MMROW(X1, W1, 3);
    CVTROW(3);
  }
  if (w >= 4) {
    const __amdgpu_buffer_rsrc_t ro = __builtin_amdgcn_make_buffer_rsrc((void*)out, 0, M_ * N_ * 2, 0x00020000);
    const int hh = w - 4;
    int lo_[4], go_[4];
#pragma unroll
    for (int i = 0; i < 4; ++i) {
      const int k = hh + 4 * i, c = k * 64 + lane, row32 = c / 28, ch = c - row32 * 28;
      const int row = (row32 >> 4) * 64 + (row32 & 15);
      lo_[i] = k < 14 ? row * OPITCH + ch * 16 : 0;
      go_[i] = k < 14 ? (m0 + row) * (N_ * 2) + n0 * 2 + ch * 16 : 0x7f000000;
    }
#pragma unroll
    for (int r = 0; r < 4; ++r) {
      while (((*(volatile LAS unsigned*)(smem + CNT_OFF) >> (8 * r)) & 0xffu) < 4u) __builtin_amdgcn_s_sleep(0);
      asm volatile("" ::: "memory");
      u32x4 v[4];
#pragma unroll
      for (int i = 0; i < 4; ++i) v[i] = *(const LAS u32x4*)(smem + lo_[i] + r * 16 * OPITCH);
#pragma unroll
      for (int i = 0; i < 4; ++i) __builtin_amdgcn_raw_buffer_store_b128(v[i], ro, go_[i] + r * 16 * (N_ * 2), 0, 2  );
    }
  }
}

extern "C" void kernel_launch(void* const* d_in, const int* in_sizes, int n_in, void* d_out, int out_size, void* d_ws, size_t ws_size,
                              hipStream_t stream) {
  const u16* x = (const u16*)d_in[0];
  const int* qw = (const int*)d_in[1];
  const u16* cb = (const u16*)d_in[2];
  const u16* am = (const u16*)d_in[3];
  (void)hipFuncSetAttribute((const void*)k_fused, hipFuncAttributeMaxDynamicSharedMemorySize, LDS_B);
  k_fused<<<256, 512, LDS_B, stream>>>(x, qw, cb, am, (char*)d_ws, (u16*)d_out);
}
```
